```python
import torch

def _softmax(x, axis=-1):
    return torch.softmax(x, dim=axis)
def _gelu(x):
    return torch.nn.functional.gelu(x, approximate="tanh")
def _silu(x):
    return torch.nn.functional.silu(x)
def _sigmoid(x):
    return torch.sigmoid(x)
def _layernorm(x, axis=-1, eps=1e-5):
    m = x.mean(dim=axis, keepdim=True)
    v = x.var(dim=axis, keepdim=True, unbiased=False)
    return (x - m) / torch.sqrt(v + eps)
def _scatter_add(tgt, idx, src, axis=0):
    return tgt.clone().index_add_(axis, idx, src)
def _segment_sum(src, seg, n):
    out = torch.zeros((n,) + src.shape[1:], dtype=src.dtype)
    return out.index_add_(0, seg, src)
def _pad(x, pw):
    flat = tuple(v for pair in reversed(pw) for v in pair)
    return torch.nn.functional.pad(x, flat)

kernel_name = 'standalone__sequential_behavior_din__recsys'

def setup_inputs():

    _w = (lambda a: (lambda t: t.to(torch.bfloat16) if t.is_floating_point() else t)(torch.as_tensor(a)))
    B, L, D = 4096, 50, 64
    H = D
    return {
        "target": _w((lambda *s: torch.randn(*s, dtype=torch.bfloat16) * 3.2)(B, D)),
        "hist": _w((lambda *s: torch.randn(*s, dtype=torch.bfloat16) * 3.2)(B, L, D)),
        "mask": _w((lambda lo, hi, *s: torch.randint(lo, hi, s))(0, 2, B, L).to(float)),
        "w1": _w((lambda *s: torch.randn(*s, dtype=torch.bfloat16) * 3.2)(4 * D, H) * (4 * D) ** -0.5),
        "b1": _w((lambda *s: torch.randn(*s, dtype=torch.bfloat16) * 3.2)(H) * 0.1),
        "w2": _w((lambda *s: torch.randn(*s, dtype=torch.bfloat16) * 0.05)(H, 1) * H ** -0.5),
        "b2": _w((lambda *s: torch.randn(*s, dtype=torch.bfloat16) * 0.05)(1) * 0.1),
    }

def reference(target, hist, mask, w1, b1, w2, b2):
    L = hist.shape[1]
    tgt = (lambda x, n, axis: torch.repeat_interleave(x, n, dim=axis))(target[:, None, :], L, axis=1)
    feat = torch.concatenate([hist, tgt, hist - tgt, hist * tgt], axis=-1)
    h = _sigmoid(feat @ w1 + b1)
    scores = (h @ w2 + b2)[..., 0] * mask
    return torch.einsum('bl,bld->bd', scores, hist)
```

```cpp
#include <hip/hip_runtime.h>
#include <hip/hip_bf16.h>
__device__ __forceinline__ float bf2f(unsigned short u){ return __uint_as_float(((unsigned)u)<<16); }
__global__ __launch_bounds__(64) void din_naive(const unsigned short* __restrict__ target,const unsigned short* __restrict__ hist,
   const unsigned short* __restrict__ mask,const unsigned short* __restrict__ w1,const unsigned short* __restrict__ b1,
   const unsigned short* __restrict__ w2,const unsigned short* __restrict__ b2,__hip_bfloat16* __restrict__ out){
  const int b=blockIdx.x, h=threadIdx.x;
  __shared__ float sh[50][64]; __shared__ float st[64];
  st[h]=bf2f(target[b*64+h]);
  for(int l=0;l<50;l++) sh[l][h]=bf2f(hist[(b*50+l)*64+h]);
  __syncthreads();
  float o=0.f;
  for(int l=0;l<50;l++){
    float acc=bf2f(b1[h]);
    for(int d=0;d<64;d++){
      float x=sh[l][d], t=st[d];
      acc+=x*bf2f(w1[d*64+h])+t*bf2f(w1[(64+d)*64+h])+(x-t)*bf2f(w1[(128+d)*64+h])+(x*t)*bf2f(w1[(192+d)*64+h]);
    }
    float s=1.f/(1.f+__expf(-acc))*bf2f(w2[h]);
    for(int o2=32;o2>0;o2>>=1) s+=__shfl_xor(s,o2,64);
    s=(s+bf2f(b2[0]))*bf2f(mask[b*50+l]);
    o+=s*sh[l][h];
  }
  out[b*64+h]=__float2bfloat16(o);
}
extern "C" void kernel_launch(void* const* d_in, const int* in_sizes, int n_in, void* d_out, int out_size, void* d_ws, size_t ws_size, hipStream_t stream){
  int B=in_sizes[0]/64;
  din_naive<<<B,64,0,stream>>>((const unsigned short*)d_in[0],(const unsigned short*)d_in[1],(const unsigned short*)d_in[2],(const unsigned short*)d_in[3],(const unsigned short*)d_in[4],(const unsigned short*)d_in[5],(const unsigned short*)d_in[6],(__hip_bfloat16*)d_out);
}
```

```cpp
#include <hip/hip_runtime.h>
#include <hip/hip_bf16.h>

typedef float f32x4 __attribute__((ext_vector_type(4)));
typedef float f32x2 __attribute__((ext_vector_type(2)));
typedef short s16x4 __attribute__((ext_vector_type(4)));
typedef short s16x8 __attribute__((ext_vector_type(8)));
typedef __bf16 bf16x8 __attribute__((ext_vector_type(8)));
typedef __bf16 bf16x2 __attribute__((ext_vector_type(2)));
typedef unsigned u32x4 __attribute__((ext_vector_type(4)));
typedef unsigned u32x2 __attribute__((ext_vector_type(2)));
#define LDS_AS __attribute__((address_space(3)))
#define DI __device__ __forceinline__

constexpr int kL = 50;
constexpr int kWaves = 16;
constexpr int kRowB = 128;
constexpr int kHistB = kL * kRowB;
constexpr int kWStride = 4096 + 16;
constexpr int OFF_WAC = 0;
constexpr int OFF_WD = OFF_WAC + 4 * kWStride;
constexpr int OFF_WBC = OFF_WD + 4 * kWStride;
constexpr int OFF_TC = OFF_WBC + 16384;
constexpr int OFF_W2 = OFF_TC + kWaves * 256;
constexpr int OFF_B2 = OFF_W2 + 256;
constexpr int OFF_LST = OFF_B2 + 64;
constexpr int OFF_HIST = ((OFF_LST + kWaves * 256) + 255) / 256 * 256;
constexpr int LDS_BYTES = OFF_HIST + kWaves * kHistB;
static_assert(LDS_BYTES <= 163840, "lds");

constexpr float kNegLog2e = -1.4426950408889634f;
constexpr float kShift = 60.0f;
constexpr float kDelta = 8.673617379884035e-19f;
constexpr float kInvDelta = 1152921504606846976.0f;

DI float lo2f(unsigned u) { return __uint_as_float(u << 16); }
DI float hi2f(unsigned u) { return __uint_as_float(u & 0xffff0000u); }
DI unsigned pk_bf16(float a, float b) {
  f32x2 v = {a, b};
  return __builtin_bit_cast(unsigned, __builtin_convertvector(v, bf16x2));
}
DI f32x4 mfma32(u32x4 a, u32x4 b, f32x4 c) {
  return __builtin_amdgcn_mfma_f32_16x16x32_bf16(__builtin_bit_cast(bf16x8, a), __builtin_bit_cast(bf16x8, b), c, 0, 0, 0);
}
DI f32x4 mfma16(u32x2 a, u32x2 b, f32x4 c) {
  return __builtin_amdgcn_mfma_f32_16x16x16bf16_1k(__builtin_bit_cast(s16x4, a), __builtin_bit_cast(s16x4, b), c, 0, 0, 0);
}
DI u32x2 tr_read(LDS_AS char* p) {
  return __builtin_bit_cast(u32x2, __builtin_amdgcn_ds_read_tr16_b64_v4i16((LDS_AS s16x4*)p));
}
template <typename T> DI T lds_ld(LDS_AS char* p) { return *(LDS_AS T*)p; }
template <typename T> DI void lds_st(LDS_AS char* p, T v) { *(LDS_AS T*)p = v; }
DI void dma16(unsigned ldsaddr, unsigned voff, __amdgpu_buffer_rsrc_t srd) {
  asm volatile("s_mov_b32 m0, %0\n\ts_nop 0\n\tbuffer_load_dwordx4 %1, %2, 0 offen lds" ::"s"(ldsaddr), "v"(voff), "s"(srd) : "m0", "memory");
}
DI f32x2 pkfma(f32x2 a, f32x2 b, f32x2 c) { return __builtin_elementwise_fma(a, b, c); }

DI float tile_scores(LDS_AS char* rowp, const u32x4 (&wb)[4][2], const f32x4 (&c)[4], const f32x4 (&w2)[4], f32x2 init, int q16) {
  const u32x4 h0 = lds_ld<u32x4>(rowp + (q16));
  const u32x4 h1 = lds_ld<u32x4>(rowp + (q16 ^ 64));
  f32x2 acc = init;
  const f32x2 dl = {kDelta, kDelta};
#pragma unroll
  for (int ht = 0; ht < 4; ++ht) {
    f32x4 y = mfma32(wb[ht][0], h0, c[ht]);
    y = mfma32(wb[ht][1], h1, y);
    f32x2 ea, eb;
    ea[0] = __builtin_amdgcn_fmed3f(__builtin_amdgcn_exp2f(y[0]), 0.f, 1.f);
    ea[1] = __builtin_amdgcn_fmed3f(__builtin_amdgcn_exp2f(y[1]), 0.f, 1.f);
    eb[0] = __builtin_amdgcn_fmed3f(__builtin_amdgcn_exp2f(y[2]), 0.f, 1.f);
    eb[1] = __builtin_amdgcn_fmed3f(__builtin_amdgcn_exp2f(y[3]), 0.f, 1.f);
    const f32x2 da = ea + dl, db = eb + dl;
    const f32x2 wa = {w2[ht][0], w2[ht][1]}, wbb = {w2[ht][2], w2[ht][3]};
    const f32x2 den = da * db;
    const f32x2 num = pkfma(wa, db, wbb * da);
    f32x2 rc;
    rc[0] = __builtin_amdgcn_rcpf(den[0]);
    rc[1] = __builtin_amdgcn_rcpf(den[1]);
    acc = pkfma(num, rc, acc);
  }
  return acc[0] + acc[1];
}

__global__ __launch_bounds__(1024) void din_kernel(const unsigned short* __restrict__ target, const unsigned short* __restrict__ hist,
                                                   const unsigned short* __restrict__ mask, const unsigned short* __restrict__ w1,
                                                   const unsigned short* __restrict__ b1, const unsigned short* __restrict__ w2g,
                                                   const unsigned short* __restrict__ b2, unsigned short* __restrict__ out) {
  __shared__ __attribute__((aligned(256))) char lds_raw[LDS_BYTES];
  LDS_AS char* lds = (LDS_AS char*)lds_raw;
  const int tid = threadIdx.x, lane = tid & 63;
  const int w = __builtin_amdgcn_readfirstlane(tid >> 6);
  const int b0 = blockIdx.x * kWaves, b = b0 + w;
  const int q = lane >> 4, r = lane & 15;

  u32x4 tf0 = {0, 0, 0, 0}, tf1 = {0, 0, 0, 0};
  u32x2 b1v = {0, 0};
  unsigned aux = 0;
  if (w < 4) {
    const unsigned toff = (unsigned)(r * 128 + q * 16);
    const unsigned boff = (unsigned)(64 * (w >> 1) + 16 * q + 8 * (w & 1));
    asm volatile("s_nop 4\n\tglobal_load_dwordx4 %0, %3, %4\n\tglobal_load_dwordx4 %1, %3, %4 offset:64\n\tglobal_load_dwordx2 %2, %5, %6"
                 : "=&v"(tf0), "=&v"(tf1), "=&v"(b1v)
                 : "v"(toff), "s"(target + (size_t)b0 * 64), "v"(boff), "s"(b1));
  } else if (w == 4) {
    asm volatile("s_nop 4\n\tglobal_load_short_d16_hi %0, %1, %2" : "+v"(aux) : "v"(lane * 2), "s"(w2g));
  } else if (w == 5) {
    asm volatile("s_nop 4\n\tglobal_load_short_d16_hi %0, %1, %2" : "+v"(aux) : "v"(0), "s"(b2));
  }
  unsigned m32 = 0, t32 = 0;
  u32x2 wa, wb_, wc, wd;
  {
    const unsigned moff = (unsigned)(min(lane, kL - 1) * 2);
    asm volatile("s_nop 4\n\tglobal_load_short_d16_hi %0, %2, %3\n\tglobal_load_short_d16_hi %1, %4, %5"
                 : "+v"(m32), "+v"(t32)
                 : "v"(moff), "s"(mask + (size_t)b * kL), "v"(lane * 2), "s"(target + (size_t)b * 64));
    const unsigned woff = (unsigned)tid * 8;
    asm volatile("global_load_dwordx2 %0, %4, %5\n\tglobal_load_dwordx2 %1, %4, %6\n\tglobal_load_dwordx2 %2, %4, %7\n\tglobal_load_dwordx2 %3, %4, %8"
                 : "=&v"(wa), "=&v"(wb_), "=&v"(wc), "=&v"(wd)
                 : "v"(woff), "s"(w1), "s"(w1 + 4096), "s"(w1 + 8192), "s"(w1 + 12288));
  }
  const unsigned ldsbase = (unsigned)(size_t)lds;
  const unsigned histl = ldsbase + OFF_HIST + w * kHistB;
  const __amdgpu_buffer_rsrc_t srd = __builtin_amdgcn_make_buffer_rsrc((void*)(hist + (size_t)b * (kL * 64)), (short)0, kHistB, 0x00020000);
  LDS_AS char* lst = lds + OFF_LST + w * 256;
  const unsigned chunkoff = (unsigned)(((lane & 7) ^ ((lane >> 3) & 6)) * 16);

  asm volatile("s_waitcnt vmcnt(5)" : "+v"(m32)::"memory");
  const bool act = (__uint_as_float(m32) != 0.f) && (lane < kL);
  const unsigned long long M = __ballot(act);
  const int n = __popcll(M);
  {
    const unsigned rk = __builtin_amdgcn_mbcnt_hi((unsigned)(M >> 32), __builtin_amdgcn_mbcnt_lo((unsigned)M, 0));
    const unsigned pos = act ? rk : (unsigned)(n + lane) - rk;
    const unsigned word = act ? (m32 | (unsigned)(lane * kRowB)) : 0x7f00u;
    lds_st<unsigned>(lst + pos * 4, word);
  }
  __builtin_amdgcn_wave_barrier();
  asm volatile("" ::: "memory");
  const int g8 = (lane >> 3) * 8;
  const int mrk = (2 * (r & 7) + (r >> 3)) * 4;
  {
    const u32x2 e0 = lds_ld<u32x2>(lst + g8), e1 = lds_ld<u32x2>(lst + 64 + g8);
    dma16(histl, (e0[0] & 0xffffu) | chunkoff, srd);
    dma16(histl + 1024, (e0[1] & 0xffffu) | chunkoff, srd);
    dma16(histl + 2048, (e1[0] & 0xffffu) | chunkoff, srd);
    dma16(histl + 3072, (e1[1] & 0xffffu) | chunkoff, srd);
  }
  asm volatile("s_waitcnt vmcnt(4)" : "+v"(t32), "+v"(wa), "+v"(wb_), "+v"(wc), "+v"(wd), "+v"(tf0), "+v"(tf1), "+v"(b1v), "+v"(aux)::"memory");
  if (n > 32) {
    const u32x2 e2 = lds_ld<u32x2>(lst + 128 + g8);
    dma16(histl + 4096, (e2[0] & 0xffffu) | chunkoff, srd);
    dma16(histl + 5120, (e2[1] & 0xffffu) | chunkoff, srd);
    if (n > 48) {
      const unsigned e3 = lds_ld<unsigned>(lst + 192 + ((lane >> 3) & 1) * 4);
      if (lane < 16) dma16(histl + 6144, (e3 & 0xffffu) | chunkoff, srd);
    }
  }
  LDS_AS char* tc = lds + OFF_TC + w * 256;
  lds_st<float>(tc + lane * 4, __uint_as_float(t32) * kNegLog2e);
  {
    const int ht = ((r >> 3) << 1) | (r & 1);
    const int vr4 = ((r >> 1) & 3) * 4;
    const int s = w >> 3, qq = (w >> 1) & 3, jh = w & 1;
    const int off = ht * kWStride + (s * 2 + jh) * 1024 + (qq * 16 + vr4) * 16 + q * 4;
    const f32x2 k2 = {kNegLog2e, kNegLog2e};
    const f32x2 a01 = {lo2f(wa[0]), hi2f(wa[0])}, a23 = {lo2f(wa[1]), hi2f(wa[1])};
    const f32x2 c01 = {lo2f(wc[0]), hi2f(wc[0])}, c23 = {lo2f(wc[1]), hi2f(wc[1])};
    const f32x2 s01 = (a01 + c01) * k2, s23 = (a23 + c23) * k2;
    lds_st<float>(lds + OFF_WAC + off, s01[0]);
    lds_st<float>(lds + OFF_WAC + off + 16, s01[1]);
    lds_st<float>(lds + OFF_WAC + off + 32, s23[0]);
    lds_st<float>(lds + OFF_WAC + off + 48, s23[1]);
    lds_st<float>(lds + OFF_WD + off, lo2f(wd[0]));
    lds_st<float>(lds + OFF_WD + off + 16, hi2f(wd[0]));
    lds_st<float>(lds + OFF_WD + off + 32, lo2f(wd[1]));
    lds_st<float>(lds + OFF_WD + off + 48, hi2f(wd[1]));
    lds_st<u32x2>(lds + OFF_WBC + tid * 8, wb_);
    lds_st<u32x2>(lds + OFF_WBC + 8192 + tid * 8, wc);
  }
  if (w == 4) lds_st<unsigned>(lds + OFF_W2 + lane * 4, aux);
  if (w == 5) { if (lane < 8) lds_st<float>(lds + OFF_B2 + lane * 4, lane == 0 ? __uint_as_float(aux) * kInvDelta : 0.f); }
  __builtin_amdgcn_wave_barrier();
  asm volatile("" ::: "memory");
  f32x4 tq[2][2];
#pragma unroll
  for (int s = 0; s < 2; ++s)
#pragma unroll
    for (int jh = 0; jh < 2; ++jh) tq[s][jh] = lds_ld<f32x4>(tc + (32 * s + 8 * q + 4 * jh) * 4);
  const u32x2 mw01 = {lds_ld<unsigned>(lst + mrk), lds_ld<unsigned>(lst + 64 + mrk)};
  asm volatile("s_waitcnt lgkmcnt(0)\n\ts_barrier" : "+v"(tq[0][0]), "+v"(tq[0][1]), "+v"(tq[1][0]), "+v"(tq[1][1])::"memory");

  if (w < 4) {
    const int p = lane & 3, qr = (lane & 15) >> 2;
    LDS_AS char* base = lds + OFF_WBC + (8 * q + qr) * 128 + (32 * (w >> 1) + 8 * p + 4 * (w & 1)) * 2;
    f32x4 accb = {0, 0, 0, 0}, accc = {0, 0, 0, 0};
#pragma unroll
    for (int s = 0; s < 2; ++s) {
      const u32x2 x0 = tr_read(base + s * 4096), x1 = tr_read(base + s * 4096 + 512);
      const u32x2 y0 = tr_read(base + 8192 + s * 4096), y1 = tr_read(base + 8192 + s * 4096 + 512);
      const u32x4 fb = {x0[0], x0[1], x1[0], x1[1]}, fc = {y0[0], y0[1], y1[0], y1[1]};
      accb = mfma32(fb, s ? tf1 : tf0, accb);
      accc = mfma32(fc, s ? tf1 : tf0, accc);
    }
    const f32x4 bb = {lo2f(b1v[0]), hi2f(b1v[0]), lo2f(b1v[1]), hi2f(b1v[1])};
    const f32x4 cv = (accb - accc + bb) * kNegLog2e - kShift;
    lds_st<f32x4>(lds + OFF_TC + r * 256 + w * 64 + q * 16, cv);
  }
  u32x4 wb[4][2];
#pragma unroll
  for (int ht = 0; ht < 4; ++ht) {
#pragma unroll
    for (int s = 0; s < 2; ++s) {
      LDS_AS char* pa = lds + OFF_WAC + ht * kWStride + s * 2048 + lane * 16;
      LDS_AS char* pd = lds + OFF_WD + ht * kWStride + s * 2048 + lane * 16;
      const f32x4 a0 = lds_ld<f32x4>(pa), a1 = lds_ld<f32x4>(pa + 1024);
      const f32x4 d0 = lds_ld<f32x4>(pd), d1 = lds_ld<f32x4>(pd + 1024);
      const f32x4 v0 = __builtin_elementwise_fma(d0, tq[s][0], a0);
      const f32x4 v1 = __builtin_elementwise_fma(d1, tq[s][1], a1);
      wb[ht][s] = u32x4{pk_bf16(v0[0], v0[1]), pk_bf16(v0[2], v0[3]), pk_bf16(v1[0], v1[1]), pk_bf16(v1[2], v1[3])};
    }
    if (ht == 0) asm volatile("s_waitcnt lgkmcnt(0)\n\ts_barrier" ::: "memory");
  }
  f32x4 c[4], w2[4];
#pragma unroll
  for (int ht = 0; ht < 4; ++ht) {
    c[ht] = lds_ld<f32x4>(tc + ht * 64 + q * 16);
    w2[ht] = lds_ld<f32x4>(lds + OFF_W2 + (32 * (ht >> 1) + 8 * q + 4 * (ht & 1)) * 4);
  }
  const f32x2 init = lds_ld<f32x2>(lds + OFF_B2 + q * 8);

  LDS_AS char* hl = lds + OFF_HIST + w * kHistB;
  const int q16 = (q ^ (r & 6)) * 16;
  const int trow = lane >> 2;
  const int toff = trow * kRowB + ((((lane >> 1) & 1) ^ (trow & 6)) * 16) + (lane & 1) * 8;
  const u32x2 sel0 = {0x00003f80u, 0x00003f80u}, sel1 = {0x3f800000u, 0x3f800000u};
  f32x4 o[4];
#pragma unroll
  for (int dt = 0; dt < 4; ++dt) o[dt] = f32x4{0, 0, 0, 0};

  auto finish_pair = [&](float p0, float p1, unsigned m0w, unsigned m1w, LDS_AS char* ta, LDS_AS char* tb, int toffa, int toffb) {
    f32x2 p = {p0, p1};
    const f32x2 mm = {hi2f(m0w) * kDelta, hi2f(m1w) * kDelta};
    p = p * mm;
    const unsigned hi = pk_bf16(p[0], p[1]);
    const f32x2 hf = {lo2f(hi), hi2f(hi)};
    const f32x2 lo = p - hf;
    const u32x2 a = {hi, pk_bf16(lo[0], lo[1])};
    const f32x4 z = {0, 0, 0, 0};
    const f32x4 z0 = mfma16(a, sel0, z), z1 = mfma16(a, sel1, z);
    const u32x4 sc = {pk_bf16(z0[0], z0[1]), pk_bf16(z0[2], z0[3]), pk_bf16(z1[0], z1[1]), pk_bf16(z1[2], z1[3])};
#pragma unroll
    for (int dt = 0; dt < 4; ++dt) {
      const u32x2 x0 = tr_read(ta + (toffa ^ (32 * dt))), x1 = tr_read(tb + (toffb ^ (32 * dt)));
      o[dt] = mfma32(sc, u32x4{x0[0], x0[1], x1[0], x1[1]}, o[dt]);
    }
  };

  asm volatile("s_waitcnt vmcnt(2)" ::: "memory");
  const float p0 = tile_scores(hl + r * kRowB, wb, c, w2, init, q16);
  asm volatile("s_waitcnt vmcnt(0)" ::: "memory");
  const float p1 = tile_scores(hl + 2048 + r * kRowB, wb, c, w2, init, q16);
  finish_pair(p0, p1, mw01[0], mw01[1], hl, hl + 2048, toff, toff);
  if (n > 32) {
    const unsigned m2 = lds_ld<unsigned>(lst + 128 + mrk);
    const float p2 = tile_scores(hl + 4096 + r * kRowB, wb, c, w2, init, q16);
    float p3 = 0.f;
    unsigned m3 = 0;
    int toff3 = toff - 2048;
    if (n > 48) {
      m3 = r < 2 ? lds_ld<unsigned>(lst + 192 + r * 4) : 0u;
      p3 = tile_scores(hl + 6144 + (r & 1) * kRowB, wb, c, w2, init, q * 16);
      toff3 = (trow & 1) * kRowB + ((lane >> 1) & 1) * 16 + (lane & 1) * 8;
    }
    finish_pair(p2, p3, m2, m3, hl + 4096, hl + 6144, toff, toff3);
  }
  const float res = q == 0 ? o[0][0] : q == 1 ? o[1][0] : q == 2 ? o[2][0] : o[3][0];
  out[(size_t)b * 64 + lane] = (unsigned short)pk_bf16(res, res);
}

extern "C" void kernel_launch(void* const* d_in, const int* in_sizes, int n_in, void* d_out, int out_size, void* d_ws, size_t ws_size,
                              hipStream_t stream) {
  const int B = in_sizes[0] / 64;
  din_kernel<<<B / kWaves, kWaves * 64, 0, stream>>>((const unsigned short*)d_in[0], (const unsigned short*)d_in[1], (const unsigned short*)d_in[2],
                                                     (const unsigned short*)d_in[3], (const unsigned short*)d_in[4], (const unsigned short*)d_in[5],
                                                     (const unsigned short*)d_in[6], (unsigned short*)d_out);
}
```

```cpp
#include <hip/hip_runtime.h>
#include <hip/hip_bf16.h>

#define STAMP(i)
#define STAMP_DECL
#define STAMP_FLUSH(wv)
#define DBG_ARG
#define DBG_PASS

#define PF_ROWS 1
#define TOUCH_OUT 1

typedef float f32x4 __attribute__((ext_vector_type(4)));
typedef float f32x2 __attribute__((ext_vector_type(2)));
typedef short s16x4 __attribute__((ext_vector_type(4)));
typedef __bf16 bf16x8 __attribute__((ext_vector_type(8)));
typedef __bf16 bf16x2 __attribute__((ext_vector_type(2)));
typedef unsigned u32x4 __attribute__((ext_vector_type(4)));
typedef unsigned u32x2 __attribute__((ext_vector_type(2)));
#define LDS_AS __attribute__((address_space(3)))
#define DI __device__ __forceinline__

constexpr int kL = 50;
constexpr int kWaves = 16;
constexpr int kRowB = 128;
constexpr int kHistB = kL * kRowB;
constexpr int kWStride = 4096 + 16;
constexpr int OFF_WAC = 0;
constexpr int OFF_WD = OFF_WAC + 4 * kWStride;
constexpr int OFF_WBC = OFF_WD + 4 * kWStride;
constexpr int OFF_TC = OFF_WBC + 16384;
constexpr int OFF_LM = OFF_TC + kWaves * 256;
constexpr int OFF_TB = OFF_LM + kWaves * 256;
constexpr int OFF_AUX = OFF_TB + kWaves * 144;
constexpr int OFF_CNT = OFF_AUX + 768;
constexpr int OFF_HIST = ((OFF_CNT + 256) + 255) / 256 * 256;
constexpr int LDS_BYTES = OFF_HIST + kWaves * kHistB;
static_assert(LDS_BYTES <= 163840, "lds");

constexpr float kNegLog2e = -1.4426950408889634f;
constexpr float kShift = 60.0f;
constexpr float kDelta = 8.673617379884035e-19f;
constexpr float kB2Scale = 72057594037927936.0f;

DI float lo2f(unsigned u) { return __uint_as_float(u << 16); }
DI float hi2f(unsigned u) { return __uint_as_float(u & 0xffff0000u); }
DI unsigned pk_bf16(f32x2 v) { return __builtin_bit_cast(unsigned, __builtin_convertvector(v, bf16x2)); }
DI f32x4 mfma32(u32x4 a, u32x4 b, f32x4 c) {
  return __builtin_amdgcn_mfma_f32_16x16x32_bf16(__builtin_bit_cast(bf16x8, a), __builtin_bit_cast(bf16x8, b), c, 0, 0, 0);
}
DI u32x2 tr_read(LDS_AS char* p) {
  return __builtin_bit_cast(u32x2, __builtin_amdgcn_ds_read_tr16_b64_v4i16((LDS_AS s16x4*)p));
}
template <typename T> DI T lds_ld(LDS_AS char* p) { return *(LDS_AS T*)p; }
template <typename T> DI T lds_ldv(LDS_AS char* p) { return *(volatile LDS_AS T*)p; }
template <typename T> DI void lds_st(LDS_AS char* p, T v) { *(LDS_AS T*)p = v; }
DI void dma16(unsigned ldsaddr, unsigned voff, __amdgpu_buffer_rsrc_t srd) {
  asm volatile("s_mov_b32 m0, %0\n\ts_nop 0\n\tbuffer_load_dwordx4 %1, %2, 0 offen nt lds" ::"s"(ldsaddr), "v"(voff), "s"(srd) : "memory");
}
DI f32x2 pkfma(f32x2 a, f32x2 b, f32x2 c) { return __builtin_elementwise_fma(a, b, c); }
DI float expc(float y) { return __builtin_amdgcn_fmed3f(__builtin_amdgcn_exp2f(y), 0.f, 1.f); }
DI f32x2 lo2(f32x4 v) { return f32x2{v[0], v[1]}; }
DI f32x2 hi2(f32x4 v) { return f32x2{v[2], v[3]}; }

DI f32x2 sig_pair(f32x2 ya, f32x2 yb, f32x2 wa, f32x2 wb, f32x2 acc) {
  const f32x2 dl = {kDelta, kDelta};
  const f32x2 da = f32x2{expc(ya[0]), expc(ya[1])} + dl;
  const f32x2 db = f32x2{expc(yb[0]), expc(yb[1])} + dl;
  const f32x2 den = da * db;
  const f32x2 num = pkfma(wa, db, wb * da);
  const f32x2 rc = {__builtin_amdgcn_rcpf(den[0]), __builtin_amdgcn_rcpf(den[1])};
  return pkfma(num, rc, acc);
}

__global__ __launch_bounds__(1024) void din_kernel(const unsigned short* __restrict__ target, const unsigned short* __restrict__ hist,
                                                   const unsigned short* __restrict__ mask, const unsigned short* __restrict__ w1,
                                                   const unsigned short* __restrict__ b1, const unsigned short* __restrict__ w2g,
                                                   const unsigned short* __restrict__ b2, unsigned short* __restrict__ out DBG_ARG) {
  STAMP_DECL
  STAMP(0);
  __shared__ __attribute__((aligned(256))) char lds_raw[LDS_BYTES];
  LDS_AS char* lds = (LDS_AS char*)lds_raw;
  const int tid = threadIdx.x, lane = tid & 63;
  const int w = __builtin_amdgcn_readfirstlane(tid >> 6);
  const int b = blockIdx.x * kWaves + w;
  const int q = lane >> 4, r = lane & 15;

  unsigned m32 = 0, t32 = 0, aux = 0;
  unsigned wa0, wa1, wb0, wb1, wc0, wc1, wd0, wd1, pf0;
  const unsigned short* histb = hist + (size_t)b * (kL * 64);
  unsigned short* outb = out + (size_t)b * 64;
  {
    const unsigned moff = (unsigned)(min(lane, kL - 1) * 2);
    const unsigned short* auxp = w == 4 ? w2g : w == 5 ? b2 : b1;
    const unsigned aoff = w == 5 ? 0u : (unsigned)lane * 2;
    asm volatile("s_nop 4\n\tglobal_load_short_d16_hi %0, %3, %4\n\tglobal_load_short_d16_hi %1, %5, %6\n\tglobal_load_short_d16_hi %2, %7, %8"
                 : "+v"(m32), "+v"(t32), "+v"(aux)
                 : "v"(moff), "s"(mask + (size_t)b * kL), "v"(lane * 2), "s"(target + (size_t)b * 64), "v"(aoff), "s"(auxp));
    const unsigned woff = (unsigned)((tid >> 5) * 256 + (tid & 31) * 4);
    asm volatile("global_load_dword %0, %8, %9\n\tglobal_load_dword %1, %8, %9 offset:128\n\t"
                 "global_load_dword %2, %8, %11\n\tglobal_load_dword %3, %8, %11 offset:128\n\t"
                 "global_load_dword %4, %8, %12\n\tglobal_load_dword %5, %8, %12 offset:128\n\t"
                 "global_load_dword %6, %8, %10\n\tglobal_load_dword %7, %8, %10 offset:128"
                 : "=&v"(wa0), "=&v"(wa1), "=&v"(wc0), "=&v"(wc1), "=&v"(wd0), "=&v"(wd1), "=&v"(wb0), "=&v"(wb1)
                 : "v"(woff), "s"(w1), "s"(w1 + 4096), "s"(w1 + 8192), "s"(w1 + 12288));
    const unsigned poff = (unsigned)(min(lane, PF_ROWS - 1) * kRowB);
    asm volatile("global_load_dword %0, %1, %2\n\tglobal_load_dword %0, %3, %4"
                 : "=&v"(pf0)
                 : "v"(poff), "s"(histb), "v"(0), "s"(TOUCH_OUT ? (const unsigned short*)outb : histb));
  }
  const unsigned ldsbase = (unsigned)(size_t)lds;
  const unsigned histl = ldsbase + OFF_HIST + w * kHistB;
  const __amdgpu_buffer_rsrc_t srd = __builtin_amdgcn_make_buffer_rsrc((void*)histb, (short)0, kHistB, 0x00020000);
  LDS_AS char* tc = lds + OFF_TC + w * 256;
  const unsigned chunk0 = (unsigned)(((lane & 7) ^ ((lane >> 3) & 2)) * 16), chunk1 = chunk0 ^ 64u;
  STAMP(1);

  asm volatile("s_waitcnt vmcnt(12)" : "+v"(m32)::"memory");
  STAMP(2);
  const unsigned long long M = __builtin_amdgcn_ballot_w64(__uint_as_float(m32) != 0.f) & ((1ull << kL) - 1);
  const bool act = __builtin_amdgcn_inverse_ballot_w64(M);
  int n = __popcll(M);
  asm volatile("" : "+s"(n));
  {
    const unsigned rk = __builtin_amdgcn_mbcnt_hi((unsigned)(M >> 32), __builtin_amdgcn_mbcnt_lo((unsigned)M, 0));
    const unsigned pos = act ? rk : (unsigned)(n + lane) - rk;
    lds_st<unsigned>(tc + pos * 4, act ? (unsigned)(lane * kRowB) : 0x7f00u);
    lds_st<unsigned>(tc + (OFF_LM - OFF_TC) + pos * 4, act ? m32 : 0u);
  }
  __builtin_amdgcn_wave_barrier();
  asm volatile("" ::: "memory");
  LDS_AS char* lo_ = tc + (lane >> 3) * 4;
  {
    const unsigned e0 = lds_ld<unsigned>(lo_), e1 = lds_ld<unsigned>(lo_ + 32), e2 = lds_ld<unsigned>(lo_ + 64), e3 = lds_ld<unsigned>(lo_ + 96);
    dma16(histl, e0 | chunk0, srd);
    dma16(histl + 1024, e1 | chunk1, srd);
    dma16(histl + 2048, e2 | chunk0, srd);
    dma16(histl + 3072, e3 | chunk1, srd);
  }
  STAMP(3);
  asm volatile("s_waitcnt vmcnt(6)"
               : "+v"(t32), "+v"(aux), "+v"(wa0), "+v"(wa1), "+v"(wb0), "+v"(wb1), "+v"(wc0), "+v"(wc1), "+v"(wd0), "+v"(wd1)::"memory");
  STAMP(4);
  if (n > 32) {
    const unsigned e4 = lds_ld<unsigned>(lo_ + 128), e5 = lds_ld<unsigned>(lo_ + 160), e6 = lds_ld<unsigned>(lo_ + 192);
    dma16(histl + 4096, e4 | chunk0, srd);
    dma16(histl + 5120, e5 | chunk1, srd);
    if (lane < 16) dma16(histl + 6144, e6 | chunk0, srd);
  }
  asm volatile("" ::: "memory");
  lds_st<float>(tc + lane * 4, __uint_as_float(t32) * kNegLog2e);
  lds_st<unsigned short>(lds + OFF_TB + w * 144 + lane * 2, (unsigned short)(t32 >> 16));
  {
    const int hp = lane & 31;
    const int s = w >> 3, qq = (w >> 1) & 3, jh = w & 1;
    const int off = (hp >> 3) * kWStride + (s * 2 + jh) * 1024 + (qq * 16 + 2 * (hp & 7)) * 16 + (lane >> 5) * 8;
    const f32x2 k2 = {kNegLog2e, kNegLog2e};
    const f32x2 s0 = (f32x2{lo2f(wa0), lo2f(wa1)} + f32x2{lo2f(wc0), lo2f(wc1)}) * k2;
    const f32x2 s1 = (f32x2{hi2f(wa0), hi2f(wa1)} + f32x2{hi2f(wc0), hi2f(wc1)}) * k2;
    lds_st<f32x2>(lds + OFF_WAC + off, s0);
    lds_st<f32x2>(lds + OFF_WAC + off + 16, s1);
    lds_st<f32x2>(lds + OFF_WD + off, f32x2{lo2f(wd0), lo2f(wd1)});
    lds_st<f32x2>(lds + OFF_WD + off + 16, f32x2{hi2f(wd0), hi2f(wd1)});
    LDS_AS char* rw = lds + OFF_WBC + (tid >> 5) * 256 + hp * 4;
    lds_st<unsigned>(rw, wb0);
    lds_st<unsigned>(rw + 128, wb1);
    lds_st<unsigned>(rw + 8192, wc0);
    lds_st<unsigned>(rw + 8192 + 128, wc1);
  }
  if ((w >> 2) == 1) {
    const float sa = w == 4 ? 1.f : w == 5 ? kB2Scale : kNegLog2e, sb = w == 6 ? -kShift : 0.f;
    const float v = __builtin_fmaf(__uint_as_float(aux), sa, sb);
    lds_st<float>(lds + OFF_AUX + (w & 3) * 256 + lane * 4, w == 7 ? 0.f : v);
  }
  __builtin_amdgcn_wave_barrier();
  asm volatile("" ::: "memory");
  f32x4 tq[2][2];
#pragma unroll
  for (int s = 0; s < 2; ++s)
#pragma unroll
    for (int jh = 0; jh < 2; ++jh) tq[s][jh] = lds_ld<f32x4>(tc + (32 * s + 8 * q + 4 * jh) * 4);
  asm volatile("s_waitcnt lgkmcnt(0)\n\ts_barrier" : "+v"(tq[0][0]), "+v"(tq[0][1]), "+v"(tq[1][0]), "+v"(tq[1][1])::"memory");
  STAMP(5);

  if (w < 4) {
    LDS_AS char* base = lds + OFF_WBC + (8 * q + (r >> 2)) * 128 + w * 32 + (lane & 3) * 8;
    f32x4 accb = {0, 0, 0, 0}, accc = {0, 0, 0, 0};
#pragma unroll
    for (int s = 0; s < 2; ++s) {
      const u32x2 x0 = tr_read(base + s * 4096), x1 = tr_read(base + s * 4096 + 512);
      const u32x2 y0 = tr_read(base + 8192 + s * 4096), y1 = tr_read(base + 8192 + s * 4096 + 512);
      const u32x4 tf = lds_ld<u32x4>(lds + OFF_TB + r * 144 + s * 64 + q * 16);
      accb = mfma32(u32x4{x0[0], x0[1], x1[0], x1[1]}, tf, accb);
      accc = mfma32(u32x4{y0[0], y0[1], y1[0], y1[1]}, tf, accc);
    }
    const f32x4 bb = lds_ld<f32x4>(lds + OFF_AUX + 512 + w * 64 + q * 16);
    const f32x4 cv = (accb - accc) * kNegLog2e + bb;
    lds_st<f32x4>(lds + OFF_TC + r * 256 + w * 64 + q * 16, cv);
    asm volatile("" ::: "memory");
    lds_st<unsigned char>(lds + OFF_CNT + w, (unsigned char)1);
    asm volatile("" ::: "memory");
  }
  u32x4 wb[4][2];
  {
    LDS_AS char* pa = lds + lane * 16;
    f32x4 a[3][2], d[3][2];
    auto ld = [&](int k) {
      const int o_ = (k >> 1) * kWStride + (k & 1) * 2048;
      a[k % 3][0] = lds_ld<f32x4>(pa + OFF_WAC + o_);
      d[k % 3][0] = lds_ld<f32x4>(pa + OFF_WD + o_);
      a[k % 3][1] = lds_ld<f32x4>(pa + OFF_WAC + o_ + 1024);
      d[k % 3][1] = lds_ld<f32x4>(pa + OFF_WD + o_ + 1024);
    };
    ld(0);
    ld(1);
#pragma unroll
    for (int k = 0; k < 8; ++k) {
      if (k + 2 < 8) ld(k + 2);
      __builtin_amdgcn_sched_barrier(0);
      const f32x4 v0 = __builtin_elementwise_fma(d[k % 3][0], tq[k & 1][0], a[k % 3][0]);
      const f32x4 v1 = __builtin_elementwise_fma(d[k % 3][1], tq[k & 1][1], a[k % 3][1]);
      wb[k >> 1][k & 1] = u32x4{pk_bf16(lo2(v0)), pk_bf16(hi2(v0)), pk_bf16(lo2(v1)), pk_bf16(hi2(v1))};
      asm volatile("" : "+v"(wb[k >> 1][k & 1]));
      __builtin_amdgcn_sched_barrier(0);
    }
  }
  f32x4 c[4];
  for (;;) {
    const unsigned cnt = lds_ldv<unsigned>(lds + OFF_CNT);
#pragma unroll
    for (int ht = 0; ht < 4; ++ht)
#pragma unroll
      for (int i = 0; i < 4; ++i) c[ht][i] = lds_ldv<float>(tc + ht * 64 + r * 4);
    if (__builtin_amdgcn_readfirstlane(cnt) == 0x01010101u) break;
  }
  f32x2 w2[4];
#pragma unroll
  for (int ht = 0; ht < 4; ++ht) w2[ht] = f32x2{lds_ldv<float>(lds + OFF_AUX + ht * 64 + r * 4), lds_ldv<float>(lds + OFF_AUX + ht * 64 + r * 4)};
  const f32x2 init = lds_ld<f32x2>(lds + OFF_AUX + 256);
  STAMP(6);

  LDS_AS char* hl = lds + OFF_HIST + w * kHistB;
  LDS_AS char* lm = lds + OFF_LM + w * 256 + q * 8;
  const int rrow = ((r & 2) << 2) | ((r >> 2) << 1) | (r & 1);
  int roffA = rrow * kRowB + ((q ^ (rrow & 2) ^ ((rrow >> 1) & 4)) * 16);
  int roffB = roffA + 2048;
  const int trow = ((lane & 8)) | (q << 1) | ((lane >> 2) & 1);
  int toffA = trow * kRowB + ((((lane >> 1) & 1) ^ (trow & 2) ^ ((trow >> 1) & 4)) * 16) + (lane & 1) * 8;
  int toffB = toffA + 2048;
  f32x4 o[4];
#pragma unroll
  for (int dt = 0; dt < 4; ++dt) o[dt] = f32x4{0, 0, 0, 0};

  int rem = n;
  asm volatile("s_waitcnt vmcnt(2)" : "+v"(pf0)::"memory");
  STAMP(7);
#pragma unroll 1
  for (;;) {
    u32x4 sc = {0, 0, 0, 0};
    {
      const u32x4 h0 = lds_ld<u32x4>(hl + roffA), h1 = lds_ld<u32x4>(hl + (roffA ^ 64));
      const f32x2 m0 = lds_ld<f32x2>(lm), m1 = lds_ld<f32x2>(lm + 32);
      f32x4 y[4];
#pragma unroll
      for (int ht = 0; ht < 4; ++ht) y[ht] = mfma32(h1, wb[ht][1], mfma32(h0, wb[ht][0], c[ht]));
      sc[0] = pk_bf16(sig_pair(lo2(y[2]), lo2(y[3]), w2[2], w2[3], sig_pair(lo2(y[0]), lo2(y[1]), w2[0], w2[1], init)) * m0);
      if (rem > 8) sc[1] = pk_bf16(sig_pair(hi2(y[2]), hi2(y[3]), w2[2], w2[3], sig_pair(hi2(y[0]), hi2(y[1]), w2[0], w2[1], init)) * m1);
    }
    STAMP(8);
    asm volatile("s_waitcnt vmcnt(0)" : "+v"(sc)::"memory");
    STAMP(9);
    if (rem > 16) {
      const u32x4 h0 = lds_ld<u32x4>(hl + roffB), h1 = lds_ld<u32x4>(hl + (roffB ^ 64));
      const f32x2 m0 = lds_ld<f32x2>(lm + 64), m1 = lds_ld<f32x2>(lm + 96);
      f32x4 y[4];
#pragma unroll
      for (int ht = 0; ht < 4; ++ht) y[ht] = mfma32(h1, wb[ht][1], mfma32(h0, wb[ht][0], c[ht]));
      sc[2] = pk_bf16(sig_pair(lo2(y[2]), lo2(y[3]), w2[2], w2[3], sig_pair(lo2(y[0]), lo2(y[1]), w2[0], w2[1], init)) * m0);
      if (rem > 24) sc[3] = pk_bf16(sig_pair(hi2(y[2]), hi2(y[3]), w2[2], w2[3], sig_pair(hi2(y[0]), hi2(y[1]), w2[0], w2[1], init)) * m1);
    }
#pragma unroll
    for (int dt = 0; dt < 4; ++dt) {
      const u32x2 x0 = tr_read(hl + (toffA ^ (32 * dt))), x1 = tr_read(hl + (toffB ^ (32 * dt)));
      o[dt] = mfma32(sc, u32x4{x0[0], x0[1], x1[0], x1[1]}, o[dt]);
    }
    rem -= 32;
    if (rem <= 0) break;
    roffA += 4096;
    toffA += 4096;
    roffB = 6144 + (r & 1) * kRowB + q * 16;
    toffB = 6144 + ((lane >> 2) & 1) * kRowB + (lane & 3) * 8;
    lm += 128;
  }
  STAMP(10);
  float s4[4];
#pragma unroll
  for (int dt = 0; dt < 4; ++dt) { const f32x2 t = lo2(o[dt]) + hi2(o[dt]); s4[dt] = t[0] + t[1]; }
  const auto x02 = __builtin_amdgcn_permlane32_swap(__float_as_uint(s4[0]), __float_as_uint(s4[2]), false, false);
  const auto x13 = __builtin_amdgcn_permlane32_swap(__float_as_uint(s4[1]), __float_as_uint(s4[3]), false, false);
  const float u0 = __uint_as_float(x02[0]) + __uint_as_float(x02[1]);
  const float u1 = __uint_as_float(x13[0]) + __uint_as_float(x13[1]);
  const auto y01 = __builtin_amdgcn_permlane16_swap(__float_as_uint(u0), __float_as_uint(u1), false, false);
  const float res = (__uint_as_float(y01[0]) + __uint_as_float(y01[1])) * kDelta;
  outb[lane] = (unsigned short)pk_bf16(f32x2{res, res});
  STAMP_FLUSH(b);
}

extern "C" void kernel_launch(void* const* d_in, const int* in_sizes, int n_in, void* d_out, int out_size, void* d_ws, size_t ws_size,
                              hipStream_t stream) {
  const int B = in_sizes[0] / 64;
  din_kernel<<<B / kWaves, kWaves * 64, 0, stream>>>((const unsigned short*)d_in[0], (const unsigned short*)d_in[1], (const unsigned short*)d_in[2],
                                                     (const unsigned short*)d_in[3], (const unsigned short*)d_in[4], (const unsigned short*)d_in[5],
                                                     (const unsigned short*)d_in[6], (unsigned short*)d_out DBG_PASS);
}
```

```cpp
#include <hip/hip_runtime.h>
#include <hip/hip_bf16.h>

#define DIAG_A 0
#define DIAG_B 1
#define DIAG_CYC 0

#define STAMP(i) do{ if ((i)==DIAG_A || (i)==DIAG_B) { unsigned long long _a; __builtin_amdgcn_sched_barrier(0); asm volatile("s_memrealtime %0\n\ts_waitcnt lgkmcnt(0)":"=s"(_a)::"memory"); __builtin_amdgcn_sched_barrier(0); if((i)==DIAG_A) ta_=_a; else tb_=_a; } }while(0)
#define STAMP_DECL unsigned long long ta_=0,tb_=0;
#define STAMP_FLUSH(wv) do{ asm volatile("s_waitcnt vmcnt(0)":::"memory"); STAMP(11); \
   if (DIAG_CYC) { unsigned long long c0=__builtin_amdgcn_s_memtime(); while(__builtin_amdgcn_s_memtime()-c0 < (unsigned long long)DIAG_CYC) __builtin_amdgcn_s_sleep(2); } \
   else { unsigned long long e=__builtin_amdgcn_s_memrealtime(); unsigned long long dl=(tb_-ta_)*10; if(dl>20000) dl=20000; while(__builtin_amdgcn_s_memrealtime()-e < dl) __builtin_amdgcn_s_sleep(2); } }while(0)
#define DBG_ARG
#define DBG_PASS
#define PF_ROWS 1
#define TOUCH_OUT 1

typedef float f32x4 __attribute__((ext_vector_type(4)));
typedef float f32x2 __attribute__((ext_vector_type(2)));
typedef short s16x4 __attribute__((ext_vector_type(4)));
typedef __bf16 bf16x8 __attribute__((ext_vector_type(8)));
typedef __bf16 bf16x2 __attribute__((ext_vector_type(2)));
typedef unsigned u32x4 __attribute__((ext_vector_type(4)));
typedef unsigned u32x2 __attribute__((ext_vector_type(2)));
#define LDS_AS __attribute__((address_space(3)))
#define DI __device__ __forceinline__

constexpr int kL = 50;
constexpr int kWaves = 16;
constexpr int kRowB = 128;
constexpr int kHistB = kL * kRowB;
constexpr int kWStride = 4096 + 16;
constexpr int OFF_WAC = 0;
constexpr int OFF_WD = OFF_WAC + 4 * kWStride;
constexpr int OFF_WBC = OFF_WD + 4 * kWStride;
constexpr int OFF_TC = OFF_WBC + 16384;
constexpr int OFF_LM = OFF_TC + kWaves * 256;
constexpr int OFF_TB = OFF_LM + kWaves * 256;
constexpr int OFF_AUX = OFF_TB + kWaves * 144;
constexpr int OFF_CNT = OFF_AUX + 768;
constexpr int OFF_HIST = ((OFF_CNT + 256) + 255) / 256 * 256;
constexpr int LDS_BYTES = OFF_HIST + kWaves * kHistB;
static_assert(LDS_BYTES <= 163840, "lds");

constexpr float kNegLog2e = -1.4426950408889634f;
constexpr float kShift = 60.0f;
constexpr float kDelta = 8.673617379884035e-19f;
constexpr float kB2Scale = 72057594037927936.0f;

DI float lo2f(unsigned u) { return __uint_as_float(u << 16); }
DI float hi2f(unsigned u) { return __uint_as_float(u & 0xffff0000u); }
DI unsigned pk_bf16(f32x2 v) { return __builtin_bit_cast(unsigned, __builtin_convertvector(v, bf16x2)); }
DI f32x4 mfma32(u32x4 a, u32x4 b, f32x4 c) {
  return __builtin_amdgcn_mfma_f32_16x16x32_bf16(__builtin_bit_cast(bf16x8, a), __builtin_bit_cast(bf16x8, b), c, 0, 0, 0);
}
DI u32x2 tr_read(LDS_AS char* p) {
  return __builtin_bit_cast(u32x2, __builtin_amdgcn_ds_read_tr16_b64_v4i16((LDS_AS s16x4*)p));
}
template <typename T> DI T lds_ld(LDS_AS char* p) { return *(LDS_AS T*)p; }
template <typename T> DI T lds_ldv(LDS_AS char* p) { return *(volatile LDS_AS T*)p; }
template <typename T> DI void lds_st(LDS_AS char* p, T v) { *(LDS_AS T*)p = v; }
DI void dma16(unsigned ldsaddr, unsigned voff, __amdgpu_buffer_rsrc_t srd) {
  asm volatile("s_mov_b32 m0, %0\n\ts_nop 0\n\tbuffer_load_dwordx4 %1, %2, 0 offen nt lds" ::"s"(ldsaddr), "v"(voff), "s"(srd) : "memory");
}
DI f32x2 pkfma(f32x2 a, f32x2 b, f32x2 c) { return __builtin_elementwise_fma(a, b, c); }
DI float expc(float y) { return __builtin_amdgcn_fmed3f(__builtin_amdgcn_exp2f(y), 0.f, 1.f); }
DI f32x2 lo2(f32x4 v) { return f32x2{v[0], v[1]}; }
DI f32x2 hi2(f32x4 v) { return f32x2{v[2], v[3]}; }

DI f32x2 sig_pair(f32x2 ya, f32x2 yb, f32x2 wa, f32x2 wb, f32x2 acc) {
  const f32x2 dl = {kDelta, kDelta};
  const f32x2 da = f32x2{expc(ya[0]), expc(ya[1])} + dl;
  const f32x2 db = f32x2{expc(yb[0]), expc(yb[1])} + dl;
  const f32x2 den = da * db;
  const f32x2 num = pkfma(wa, db, wb * da);
  const f32x2 rc = {__builtin_amdgcn_rcpf(den[0]), __builtin_amdgcn_rcpf(den[1])};
  return pkfma(num, rc, acc);
}

__global__ __launch_bounds__(1024) void din_kernel(const unsigned short* __restrict__ target, const unsigned short* __restrict__ hist,
                                                   const unsigned short* __restrict__ mask, const unsigned short* __restrict__ w1,
                                                   const unsigned short* __restrict__ b1, const unsigned short* __restrict__ w2g,
                                                   const unsigned short* __restrict__ b2, unsigned short* __restrict__ out DBG_ARG) {
  STAMP_DECL
  STAMP(0);
  __shared__ __attribute__((aligned(256))) char lds_raw[LDS_BYTES];
  LDS_AS char* lds = (LDS_AS char*)lds_raw;
  const int tid = threadIdx.x, lane = tid & 63;
  const int w = __builtin_amdgcn_readfirstlane(tid >> 6);
  const int b = blockIdx.x * kWaves + w;
  const int q = lane >> 4, r = lane & 15;

  unsigned m32 = 0, t32 = 0, aux = 0;
  unsigned wa0, wa1, wb0, wb1, wc0, wc1, wd0, wd1, pf0;
  const unsigned short* histb = hist + (size_t)b * (kL * 64);
  unsigned short* outb = out + (size_t)b * 64;
  {
    const unsigned moff = (unsigned)(min(lane, kL - 1) * 2);
    const unsigned short* auxp = w == 4 ? w2g : w == 5 ? b2 : b1;
    const unsigned aoff = w == 5 ? 0u : (unsigned)lane * 2;
    asm volatile("s_nop 4\n\tglobal_load_short_d16_hi %0, %3, %4\n\tglobal_load_short_d16_hi %1, %5, %6\n\tglobal_load_short_d16_hi %2, %7, %8"
                 : "+v"(m32), "+v"(t32), "+v"(aux)
                 : "v"(moff), "s"(mask + (size_t)b * kL), "v"(lane * 2), "s"(target + (size_t)b * 64), "v"(aoff), "s"(auxp));
    const unsigned woff = (unsigned)((tid >> 5) * 256 + (tid & 31) * 4);
    asm volatile("global_load_dword %0, %8, %9\n\tglobal_load_dword %1, %8, %9 offset:128\n\t"
                 "global_load_dword %2, %8, %11\n\tglobal_load_dword %3, %8, %11 offset:128\n\t"
                 "global_load_dword %4, %8, %12\n\tglobal_load_dword %5, %8, %12 offset:128\n\t"
                 "global_load_dword %6, %8, %10\n\tglobal_load_dword %7, %8, %10 offset:128"
                 : "=&v"(wa0), "=&v"(wa1), "=&v"(wc0), "=&v"(wc1), "=&v"(wd0), "=&v"(wd1), "=&v"(wb0), "=&v"(wb1)
                 : "v"(woff), "s"(w1), "s"(w1 + 4096), "s"(w1 + 8192), "s"(w1 + 12288));
    const unsigned poff = (unsigned)(min(lane, PF_ROWS - 1) * kRowB);
    asm volatile("global_load_dword %0, %1, %2\n\tglobal_load_dword %0, %3, %4"
                 : "=&v"(pf0)
                 : "v"(poff), "s"(histb), "v"(0), "s"(TOUCH_OUT ? (const unsigned short*)outb : histb));
  }
  const unsigned ldsbase = (unsigned)(size_t)lds;
  const unsigned histl = ldsbase + OFF_HIST + w * kHistB;
  const __amdgpu_buffer_rsrc_t srd = __builtin_amdgcn_make_buffer_rsrc((void*)histb, (short)0, kHistB, 0x00020000);
  LDS_AS char* tc = lds + OFF_TC + w * 256;
  const unsigned chunk0 = (unsigned)(((lane & 7) ^ ((lane >> 3) & 2)) * 16), chunk1 = chunk0 ^ 64u;
  STAMP(1);

  asm volatile("s_waitcnt vmcnt(12)" : "+v"(m32)::"memory");
  STAMP(2);
  const unsigned long long M = __builtin_amdgcn_ballot_w64(__uint_as_float(m32) != 0.f) & ((1ull << kL) - 1);
  const bool act = __builtin_amdgcn_inverse_ballot_w64(M);
  int n = __popcll(M);
  asm volatile("" : "+s"(n));
  {
    const unsigned rk = __builtin_amdgcn_mbcnt_hi((unsigned)(M >> 32), __builtin_amdgcn_mbcnt_lo((unsigned)M, 0));
    const unsigned pos = act ? rk : (unsigned)(n + lane) - rk;
    lds_st<unsigned>(tc + pos * 4, act ? (unsigned)(lane * kRowB) : 0x7f00u);
    lds_st<unsigned>(tc + (OFF_LM - OFF_TC) + pos * 4, act ? m32 : 0u);
  }
  __builtin_amdgcn_wave_barrier();
  asm volatile("" ::: "memory");
  LDS_AS char* lo_ = tc + (lane >> 3) * 4;
  {
    const unsigned e0 = lds_ld<unsigned>(lo_), e1 = lds_ld<unsigned>(lo_ + 32), e2 = lds_ld<unsigned>(lo_ + 64), e3 = lds_ld<unsigned>(lo_ + 96);
    dma16(histl, e0 | chunk0, srd);
    dma16(histl + 1024, e1 | chunk1, srd);
    dma16(histl + 2048, e2 | chunk0, srd);
    dma16(histl + 3072, e3 | chunk1, srd);
  }
  STAMP(3);
  asm volatile("s_waitcnt vmcnt(6)"
               : "+v"(t32), "+v"(aux), "+v"(wa0), "+v"(wa1), "+v"(wb0), "+v"(wb1), "+v"(wc0), "+v"(wc1), "+v"(wd0), "+v"(wd1)::"memory");
  STAMP(4);
  if (n > 32) {
    const unsigned e4 = lds_ld<unsigned>(lo_ + 128), e5 = lds_ld<unsigned>(lo_ + 160), e6 = lds_ld<unsigned>(lo_ + 192);
    dma16(histl + 4096, e4 | chunk0, srd);
    dma16(histl + 5120, e5 | chunk1, srd);
    if (lane < 16) dma16(histl + 6144, e6 | chunk0, srd);
  }
  asm volatile("" ::: "memory");
  lds_st<float>(tc + lane * 4, __uint_as_float(t32) * kNegLog2e);
  lds_st<unsigned short>(lds + OFF_TB + w * 144 + lane * 2, (unsigned short)(t32 >> 16));
  {
    const int hp = lane & 31;
    const int s = w >> 3, qq = (w >> 1) & 3, jh = w & 1;
    const int off = (hp >> 3) * kWStride + (s * 2 + jh) * 1024 + (qq * 16 + 2 * (hp & 7)) * 16 + (lane >> 5) * 8;
    const f32x2 k2 = {kNegLog2e, kNegLog2e};
    const f32x2 s0 = (f32x2{lo2f(wa0), lo2f(wa1)} + f32x2{lo2f(wc0), lo2f(wc1)}) * k2;
    const f32x2 s1 = (f32x2{hi2f(wa0), hi2f(wa1)} + f32x2{hi2f(wc0), hi2f(wc1)}) * k2;
    lds_st<f32x2>(lds + OFF_WAC + off, s0);
    lds_st<f32x2>(lds + OFF_WAC + off + 16, s1);
    lds_st<f32x2>(lds + OFF_WD + off, f32x2{lo2f(wd0), lo2f(wd1)});
    lds_st<f32x2>(lds + OFF_WD + off + 16, f32x2{hi2f(wd0), hi2f(wd1)});
    LDS_AS char* rw = lds + OFF_WBC + (tid >> 5) * 256 + hp * 4;
    lds_st<unsigned>(rw, wb0);
    lds_st<unsigned>(rw + 128, wb1);
    lds_st<unsigned>(rw + 8192, wc0);
    lds_st<unsigned>(rw + 8192 + 128, wc1);
  }
  if ((w >> 2) == 1) {
    const float sa = w == 4 ? 1.f : w == 5 ? kB2Scale : kNegLog2e, sb = w == 6 ? -kShift : 0.f;
    const float v = __builtin_fmaf(__uint_as_float(aux), sa, sb);
    lds_st<float>(lds + OFF_AUX + (w & 3) * 256 + lane * 4, w == 7 ? 0.f : v);
  }
  __builtin_amdgcn_wave_barrier();
  asm volatile("" ::: "memory");
  f32x4 tq[2][2];
#pragma unroll
  for (int s = 0; s < 2; ++s)
#pragma unroll
    for (int jh = 0; jh < 2; ++jh) tq[s][jh] = lds_ld<f32x4>(tc + (32 * s + 8 * q + 4 * jh) * 4);
  asm volatile("s_waitcnt lgkmcnt(0)\n\ts_barrier" : "+v"(tq[0][0]), "+v"(tq[0][1]), "+v"(tq[1][0]), "+v"(tq[1][1])::"memory");
  STAMP(5);

  if (w < 4) {
    LDS_AS char* base = lds + OFF_WBC + (8 * q + (r >> 2)) * 128 + w * 32 + (lane & 3) * 8;
    f32x4 accb = {0, 0, 0, 0}, accc = {0, 0, 0, 0};
#pragma unroll
    for (int s = 0; s < 2; ++s) {
      const u32x2 x0 = tr_read(base + s * 4096), x1 = tr_read(base + s * 4096 + 512);
      const u32x2 y0 = tr_read(base + 8192 + s * 4096), y1 = tr_read(base + 8192 + s * 4096 + 512);
      const u32x4 tf = lds_ld<u32x4>(lds + OFF_TB + r * 144 + s * 64 + q * 16);
      accb = mfma32(u32x4{x0[0], x0[1], x1[0], x1[1]}, tf, accb);
      accc = mfma32(u32x4{y0[0], y0[1], y1[0], y1[1]}, tf, accc);
    }
    const f32x4 bb = lds_ld<f32x4>(lds + OFF_AUX + 512 + w * 64 + q * 16);
    const f32x4 cv = (accb - accc) * kNegLog2e + bb;
    lds_st<f32x4>(lds + OFF_TC + r * 256 + w * 64 + q * 16, cv);
    asm volatile("" ::: "memory");
    lds_st<unsigned char>(lds + OFF_CNT + w, (unsigned char)1);
    asm volatile("" ::: "memory");
  }
  u32x4 wb[4][2];
  {
    LDS_AS char* pa = lds + lane * 16;
    f32x4 a[3][2], d[3][2];
    auto ld = [&](int k) {
      const int o_ = (k >> 1) * kWStride + (k & 1) * 2048;
      a[k % 3][0] = lds_ld<f32x4>(pa + OFF_WAC + o_);
      d[k % 3][0] = lds_ld<f32x4>(pa + OFF_WD + o_);
      a[k % 3][1] = lds_ld<f32x4>(pa + OFF_WAC + o_ + 1024);
      d[k % 3][1] = lds_ld<f32x4>(pa + OFF_WD + o_ + 1024);
    };
    ld(0);
    ld(1);
#pragma unroll
    for (int k = 0; k < 8; ++k) {
      if (k + 2 < 8) ld(k + 2);
      __builtin_amdgcn_sched_barrier(0);
      const f32x4 v0 = __builtin_elementwise_fma(d[k % 3][0], tq[k & 1][0], a[k % 3][0]);
      const f32x4 v1 = __builtin_elementwise_fma(d[k % 3][1], tq[k & 1][1], a[k % 3][1]);
      wb[k >> 1][k & 1] = u32x4{pk_bf16(lo2(v0)), pk_bf16(hi2(v0)), pk_bf16(lo2(v1)), pk_bf16(hi2(v1))};
      asm volatile("" : "+v"(wb[k >> 1][k & 1]));
      __builtin_amdgcn_sched_barrier(0);
    }
  }
  f32x4 c[4];
  for (;;) {
    const unsigned cnt = lds_ldv<unsigned>(lds + OFF_CNT);
#pragma unroll
    for (int ht = 0; ht < 4; ++ht)
#pragma unroll
      for (int i = 0; i < 4; ++i) c[ht][i] = lds_ldv<float>(tc + ht * 64 + r * 4);
    if (__builtin_amdgcn_readfirstlane(cnt) == 0x01010101u) break;
  }
  f32x2 w2[4];
#pragma unroll
  for (int ht = 0; ht < 4; ++ht) w2[ht] = f32x2{lds_ldv<float>(lds + OFF_AUX + ht * 64 + r * 4), lds_ldv<float>(lds + OFF_AUX + ht * 64 + r * 4)};
  const f32x2 init = lds_ld<f32x2>(lds + OFF_AUX + 256);
  STAMP(6);

  LDS_AS char* hl = lds + OFF_HIST + w * kHistB;
  LDS_AS char* lm = lds + OFF_LM + w * 256 + q * 8;
  const int rrow = ((r & 2) << 2) | ((r >> 2) << 1) | (r & 1);
  int roffA = rrow * kRowB + ((q ^ (rrow & 2) ^ ((rrow >> 1) & 4)) * 16);
  int roffB = roffA + 2048;
  const int trow = ((lane & 8)) | (q << 1) | ((lane >> 2) & 1);
  int toffA = trow * kRowB + ((((lane >> 1) & 1) ^ (trow & 2) ^ ((trow >> 1) & 4)) * 16) + (lane & 1) * 8;
  int toffB = toffA + 2048;
  f32x4 o[4];
#pragma unroll
  for (int dt = 0; dt < 4; ++dt) o[dt] = f32x4{0, 0, 0, 0};

  int rem = n;
  asm volatile("s_waitcnt vmcnt(2)" : "+v"(pf0)::"memory");
  STAMP(7);
#pragma unroll 1
  for (;;) {
    u32x4 sc = {0, 0, 0, 0};
    {
      const u32x4 h0 = lds_ld<u32x4>(hl + roffA), h1 = lds_ld<u32x4>(hl + (roffA ^ 64));
      const f32x2 m0 = lds_ld<f32x2>(lm), m1 = lds_ld<f32x2>(lm + 32);
      f32x4 y[4];
#pragma unroll
      for (int ht = 0; ht < 4; ++ht) y[ht] = mfma32(h1, wb[ht][1], mfma32(h0, wb[ht][0], c[ht]));
      sc[0] = pk_bf16(sig_pair(lo2(y[2]), lo2(y[3]), w2[2], w2[3], sig_pair(lo2(y[0]), lo2(y[1]), w2[0], w2[1], init)) * m0);
      if (rem > 8) sc[1] = pk_bf16(sig_pair(hi2(y[2]), hi2(y[3]), w2[2], w2[3], sig_pair(hi2(y[0]), hi2(y[1]), w2[0], w2[1], init)) * m1);
    }
    STAMP(8);
    asm volatile("s_waitcnt vmcnt(0)" : "+v"(sc)::"memory");
    STAMP(9);
    if (rem > 16) {
      const u32x4 h0 = lds_ld<u32x4>(hl + roffB), h1 = lds_ld<u32x4>(hl + (roffB ^ 64));
      const f32x2 m0 = lds_ld<f32x2>(lm + 64), m1 = lds_ld<f32x2>(lm + 96);
      f32x4 y[4];
#pragma unroll
      for (int ht = 0; ht < 4; ++ht) y[ht] = mfma32(h1, wb[ht][1], mfma32(h0, wb[ht][0], c[ht]));
      sc[2] = pk_bf16(sig_pair(lo2(y[2]), lo2(y[3]), w2[2], w2[3], sig_pair(lo2(y[0]), lo2(y[1]), w2[0], w2[1], init)) * m0);
      if (rem > 24) sc[3] = pk_bf16(sig_pair(hi2(y[2]), hi2(y[3]), w2[2], w2[3], sig_pair(hi2(y[0]), hi2(y[1]), w2[0], w2[1], init)) * m1);
    }
#pragma unroll
    for (int dt = 0; dt < 4; ++dt) {
      const u32x2 x0 = tr_read(hl + (toffA ^ (32 * dt))), x1 = tr_read(hl + (toffB ^ (32 * dt)));
      o[dt] = mfma32(sc, u32x4{x0[0], x0[1], x1[0], x1[1]}, o[dt]);
    }
    rem -= 32;
    if (rem <= 0) break;
    roffA += 4096;
    toffA += 4096;
    roffB = 6144 + (r & 1) * kRowB + q * 16;
    toffB = 6144 + ((lane >> 2) & 1) * kRowB + (lane & 3) * 8;
    lm += 128;
  }
  STAMP(10);
  float s4[4];
#pragma unroll
  for (int dt = 0; dt < 4; ++dt) { const f32x2 t = lo2(o[dt]) + hi2(o[dt]); s4[dt] = t[0] + t[1]; }
  const auto x02 = __builtin_amdgcn_permlane32_swap(__float_as_uint(s4[0]), __float_as_uint(s4[2]), false, false);
  const auto x13 = __builtin_amdgcn_permlane32_swap(__float_as_uint(s4[1]), __float_as_uint(s4[3]), false, false);
  const float u0 = __uint_as_float(x02[0]) + __uint_as_float(x02[1]);
  const float u1 = __uint_as_float(x13[0]) + __uint_as_float(x13[1]);
  const auto y01 = __builtin_amdgcn_permlane16_swap(__float_as_uint(u0), __float_as_uint(u1), false, false);
  const float res = (__uint_as_float(y01[0]) + __uint_as_float(y01[1])) * kDelta;
  outb[lane] = (unsigned short)pk_bf16(f32x2{res, res});
  STAMP_FLUSH(b);
}

extern "C" void kernel_launch(void* const* d_in, const int* in_sizes, int n_in, void* d_out, int out_size, void* d_ws, size_t ws_size,
                              hipStream_t stream) {
  const int B = in_sizes[0] / 64;
  din_kernel<<<B / kWaves, kWaves * 64, 0, stream>>>((const unsigned short*)d_in[0], (const unsigned short*)d_in[1], (const unsigned short*)d_in[2],
                                                     (const unsigned short*)d_in[3], (const unsigned short*)d_in[4], (const unsigned short*)d_in[5],
                                                     (const unsigned short*)d_in[6], (unsigned short*)d_out DBG_PASS);
}
```

```cpp
#include <hip/hip_runtime.h>
#include <hip/hip_bf16.h>

#define DIAG_X 7

#define STAMP(i) do{ if ((i)==0) { ta_=__builtin_amdgcn_s_memrealtime(); } }while(0)
#define STAMP_DECL unsigned long long ta_=0;
#define STAMP_FLUSH(wv) do{ unsigned xcc; asm volatile("s_getreg_b32 %0, hwreg(HW_REG_XCC_ID)":"=s"(xcc)); \
   if (DIAG_X < 0 || (int)(xcc&15)==DIAG_X) { while(__builtin_amdgcn_s_memrealtime()-ta_ < 3000) __builtin_amdgcn_s_sleep(2); } }while(0)
#define DBG_ARG
#define DBG_PASS
#define PF_ROWS 1
#define TOUCH_OUT 1

typedef float f32x4 __attribute__((ext_vector_type(4)));
typedef float f32x2 __attribute__((ext_vector_type(2)));
typedef short s16x4 __attribute__((ext_vector_type(4)));
typedef __bf16 bf16x8 __attribute__((ext_vector_type(8)));
typedef __bf16 bf16x2 __attribute__((ext_vector_type(2)));
typedef unsigned u32x4 __attribute__((ext_vector_type(4)));
typedef unsigned u32x2 __attribute__((ext_vector_type(2)));
#define LDS_AS __attribute__((address_space(3)))
#define DI __device__ __forceinline__

constexpr int kL = 50;
constexpr int kWaves = 16;
constexpr int kRowB = 128;
constexpr int kHistB = kL * kRowB;
constexpr int kWStride = 4096 + 16;
constexpr int OFF_WAC = 0;
constexpr int OFF_WD = OFF_WAC + 4 * kWStride;
constexpr int OFF_WBC = OFF_WD + 4 * kWStride;
constexpr int OFF_TC = OFF_WBC + 16384;
constexpr int OFF_LM = OFF_TC + kWaves * 256;
constexpr int OFF_TB = OFF_LM + kWaves * 256;
constexpr int OFF_AUX = OFF_TB + kWaves * 144;
constexpr int OFF_CNT = OFF_AUX + 768;
constexpr int OFF_HIST = ((OFF_CNT + 256) + 255) / 256 * 256;
constexpr int LDS_BYTES = OFF_HIST + kWaves * kHistB;
static_assert(LDS_BYTES <= 163840, "lds");

constexpr float kNegLog2e = -1.4426950408889634f;
constexpr float kShift = 60.0f;
constexpr float kDelta = 8.673617379884035e-19f;
constexpr float kB2Scale = 72057594037927936.0f;

DI float lo2f(unsigned u) { return __uint_as_float(u << 16); }
DI float hi2f(unsigned u) { return __uint_as_float(u & 0xffff0000u); }
DI unsigned pk_bf16(f32x2 v) { return __builtin_bit_cast(unsigned, __builtin_convertvector(v, bf16x2)); }
DI f32x4 mfma32(u32x4 a, u32x4 b, f32x4 c) {
  return __builtin_amdgcn_mfma_f32_16x16x32_bf16(__builtin_bit_cast(bf16x8, a), __builtin_bit_cast(bf16x8, b), c, 0, 0, 0);
}
DI u32x2 tr_read(LDS_AS char* p) {
  return __builtin_bit_cast(u32x2, __builtin_amdgcn_ds_read_tr16_b64_v4i16((LDS_AS s16x4*)p));
}
template <typename T> DI T lds_ld(LDS_AS char* p) { return *(LDS_AS T*)p; }
template <typename T> DI T lds_ldv(LDS_AS char* p) { return *(volatile LDS_AS T*)p; }
template <typename T> DI void lds_st(LDS_AS char* p, T v) { *(LDS_AS T*)p = v; }
DI void dma16(unsigned ldsaddr, unsigned voff, __amdgpu_buffer_rsrc_t srd) {
  asm volatile("s_mov_b32 m0, %0\n\ts_nop 0\n\tbuffer_load_dwordx4 %1, %2, 0 offen nt lds" ::"s"(ldsaddr), "v"(voff), "s"(srd) : "memory");
}
DI f32x2 pkfma(f32x2 a, f32x2 b, f32x2 c) { return __builtin_elementwise_fma(a, b, c); }
DI float expc(float y) { return __builtin_amdgcn_fmed3f(__builtin_amdgcn_exp2f(y), 0.f, 1.f); }
DI f32x2 lo2(f32x4 v) { return f32x2{v[0], v[1]}; }
DI f32x2 hi2(f32x4 v) { return f32x2{v[2], v[3]}; }

DI f32x2 sig_pair(f32x2 ya, f32x2 yb, f32x2 wa, f32x2 wb, f32x2 acc) {
  const f32x2 dl = {kDelta, kDelta};
  const f32x2 da = f32x2{expc(ya[0]), expc(ya[1])} + dl;
  const f32x2 db = f32x2{expc(yb[0]), expc(yb[1])} + dl;
  const f32x2 den = da * db;
  const f32x2 num = pkfma(wa, db, wb * da);
  const f32x2 rc = {__builtin_amdgcn_rcpf(den[0]), __builtin_amdgcn_rcpf(den[1])};
  return pkfma(num, rc, acc);
}

__global__ __launch_bounds__(1024) void din_kernel(const unsigned short* __restrict__ target, const unsigned short* __restrict__ hist,
                                                   const unsigned short* __restrict__ mask, const unsigned short* __restrict__ w1,
                                                   const unsigned short* __restrict__ b1, const unsigned short* __restrict__ w2g,
                                                   const unsigned short* __restrict__ b2, unsigned short* __restrict__ out DBG_ARG) {
  STAMP_DECL
  STAMP(0);
  __shared__ __attribute__((aligned(256))) char lds_raw[LDS_BYTES];
  LDS_AS char* lds = (LDS_AS char*)lds_raw;
  const int tid = threadIdx.x, lane = tid & 63;
  const int w = __builtin_amdgcn_readfirstlane(tid >> 6);
  const int b = blockIdx.x * kWaves + w;
  const int q = lane >> 4, r = lane & 15;

  unsigned m32 = 0, t32 = 0, aux = 0;
  unsigned wa0, wa1, wb0, wb1, wc0, wc1, wd0, wd1, pf0;
  const unsigned short* histb = hist + (size_t)b * (kL * 64);
  unsigned short* outb = out + (size_t)b * 64;
  {
    const unsigned moff = (unsigned)(min(lane, kL - 1) * 2);
    const unsigned short* auxp = w == 4 ? w2g : w == 5 ? b2 : b1;
    const unsigned aoff = w == 5 ? 0u : (unsigned)lane * 2;
    asm volatile("s_nop 4\n\tglobal_load_short_d16_hi %0, %3, %4\n\tglobal_load_short_d16_hi %1, %5, %6\n\tglobal_load_short_d16_hi %2, %7, %8"
                 : "+v"(m32), "+v"(t32), "+v"(aux)
                 : "v"(moff), "s"(mask + (size_t)b * kL), "v"(lane * 2), "s"(target + (size_t)b * 64), "v"(aoff), "s"(auxp));
    const unsigned woff = (unsigned)((tid >> 5) * 256 + (tid & 31) * 4);
    asm volatile("global_load_dword %0, %8, %9\n\tglobal_load_dword %1, %8, %9 offset:128\n\t"
                 "global_load_dword %2, %8, %11\n\tglobal_load_dword %3, %8, %11 offset:128\n\t"
                 "global_load_dword %4, %8, %12\n\tglobal_load_dword %5, %8, %12 offset:128\n\t"
                 "global_load_dword %6, %8, %10\n\tglobal_load_dword %7, %8, %10 offset:128"
                 : "=&v"(wa0), "=&v"(wa1), "=&v"(wc0), "=&v"(wc1), "=&v"(wd0), "=&v"(wd1), "=&v"(wb0), "=&v"(wb1)
                 : "v"(woff), "s"(w1), "s"(w1 + 4096), "s"(w1 + 8192), "s"(w1 + 12288));
    const unsigned poff = (unsigned)(min(lane, PF_ROWS - 1) * kRowB);
    asm volatile("global_load_dword %0, %1, %2\n\tglobal_load_dword %0, %3, %4"
                 : "=&v"(pf0)
                 : "v"(poff), "s"(histb), "v"(0), "s"(TOUCH_OUT ? (const unsigned short*)outb : histb));
  }
  const unsigned ldsbase = (unsigned)(size_t)lds;
  const unsigned histl = ldsbase + OFF_HIST + w * kHistB;
  const __amdgpu_buffer_rsrc_t srd = __builtin_amdgcn_make_buffer_rsrc((void*)histb, (short)0, kHistB, 0x00020000);
  LDS_AS char* tc = lds + OFF_TC + w * 256;
  const unsigned chunk0 = (unsigned)(((lane & 7) ^ ((lane >> 3) & 2)) * 16), chunk1 = chunk0 ^ 64u;
  STAMP(1);

  asm volatile("s_waitcnt vmcnt(12)" : "+v"(m32)::"memory");
  STAMP(2);
  const unsigned long long M = __builtin_amdgcn_ballot_w64(__uint_as_float(m32) != 0.f) & ((1ull << kL) - 1);
  const bool act = __builtin_amdgcn_inverse_ballot_w64(M);
  int n = __popcll(M);
  asm volatile("" : "+s"(n));
  {
    const unsigned rk = __builtin_amdgcn_mbcnt_hi((unsigned)(M >> 32), __builtin_amdgcn_mbcnt_lo((unsigned)M, 0));
    const unsigned pos = act ? rk : (unsigned)(n + lane) - rk;
    lds_st<unsigned>(tc + pos * 4, act ? (unsigned)(lane * kRowB) : 0x7f00u);
    lds_st<unsigned>(tc + (OFF_LM - OFF_TC) + pos * 4, act ? m32 : 0u);
  }
  __builtin_amdgcn_wave_barrier();
  asm volatile("" ::: "memory");
  LDS_AS char* lo_ = tc + (lane >> 3) * 4;
  {
    const unsigned e0 = lds_ld<unsigned>(lo_), e1 = lds_ld<unsigned>(lo_ + 32), e2 = lds_ld<unsigned>(lo_ + 64), e3 = lds_ld<unsigned>(lo_ + 96);
    dma16(histl, e0 | chunk0, srd);
    dma16(histl + 1024, e1 | chunk1, srd);
    dma16(histl + 2048, e2 | chunk0, srd);
    dma16(histl + 3072, e3 | chunk1, srd);
  }
  STAMP(3);
  asm volatile("s_waitcnt vmcnt(6)"
               : "+v"(t32), "+v"(aux), "+v"(wa0), "+v"(wa1), "+v"(wb0), "+v"(wb1), "+v"(wc0), "+v"(wc1), "+v"(wd0), "+v"(wd1)::"memory");
  STAMP(4);
  if (n > 32) {
    const unsigned e4 = lds_ld<unsigned>(lo_ + 128), e5 = lds_ld<unsigned>(lo_ + 160), e6 = lds_ld<unsigned>(lo_ + 192);
    dma16(histl + 4096, e4 | chunk0, srd);
    dma16(histl + 5120, e5 | chunk1, srd);
    if (lane < 16) dma16(histl + 6144, e6 | chunk0, srd);
  }
  asm volatile("" ::: "memory");
  lds_st<float>(tc + lane * 4, __uint_as_float(t32) * kNegLog2e);
  lds_st<unsigned short>(lds + OFF_TB + w * 144 + lane * 2, (unsigned short)(t32 >> 16));
  {
    const int hp = lane & 31;
    const int s = w >> 3, qq = (w >> 1) & 3, jh = w & 1;
    const int off = (hp >> 3) * kWStride + (s * 2 + jh) * 1024 + (qq * 16 + 2 * (hp & 7)) * 16 + (lane >> 5) * 8;
    const f32x2 k2 = {kNegLog2e, kNegLog2e};
    const f32x2 s0 = (f32x2{lo2f(wa0), lo2f(wa1)} + f32x2{lo2f(wc0), lo2f(wc1)}) * k2;
    const f32x2 s1 = (f32x2{hi2f(wa0), hi2f(wa1)} + f32x2{hi2f(wc0), hi2f(wc1)}) * k2;
    lds_st<f32x2>(lds + OFF_WAC + off, s0);
    lds_st<f32x2>(lds + OFF_WAC + off + 16, s1);
    lds_st<f32x2>(lds + OFF_WD + off, f32x2{lo2f(wd0), lo2f(wd1)});
    lds_st<f32x2>(lds + OFF_WD + off + 16, f32x2{hi2f(wd0), hi2f(wd1)});
    LDS_AS char* rw = lds + OFF_WBC + (tid >> 5) * 256 + hp * 4;
    lds_st<unsigned>(rw, wb0);
    lds_st<unsigned>(rw + 128, wb1);
    lds_st<unsigned>(rw + 8192, wc0);
    lds_st<unsigned>(rw + 8192 + 128, wc1);
  }
  if ((w >> 2) == 1) {
    const float sa = w == 4 ? 1.f : w == 5 ? kB2Scale : kNegLog2e, sb = w == 6 ? -kShift : 0.f;
    const float v = __builtin_fmaf(__uint_as_float(aux), sa, sb);
    lds_st<float>(lds + OFF_AUX + (w & 3) * 256 + lane * 4, w == 7 ? 0.f : v);
  }
  __builtin_amdgcn_wave_barrier();
  asm volatile("" ::: "memory");
  f32x4 tq[2][2];
#pragma unroll
  for (int s = 0; s < 2; ++s)
#pragma unroll
    for (int jh = 0; jh < 2; ++jh) tq[s][jh] = lds_ld<f32x4>(tc + (32 * s + 8 * q + 4 * jh) * 4);
  asm volatile("s_waitcnt lgkmcnt(0)\n\ts_barrier" : "+v"(tq[0][0]), "+v"(tq[0][1]), "+v"(tq[1][0]), "+v"(tq[1][1])::"memory");
  STAMP(5);

  if (w < 4) {
    LDS_AS char* base = lds + OFF_WBC + (8 * q + (r >> 2)) * 128 + w * 32 + (lane & 3) * 8;
    f32x4 accb = {0, 0, 0, 0}, accc = {0, 0, 0, 0};
#pragma unroll
    for (int s = 0; s < 2; ++s) {
      const u32x2 x0 = tr_read(base + s * 4096), x1 = tr_read(base + s * 4096 + 512);
      const u32x2 y0 = tr_read(base + 8192 + s * 4096), y1 = tr_read(base + 8192 + s * 4096 + 512);
      const u32x4 tf = lds_ld<u32x4>(lds + OFF_TB + r * 144 + s * 64 + q * 16);
      accb = mfma32(u32x4{x0[0], x0[1], x1[0], x1[1]}, tf, accb);
      accc = mfma32(u32x4{y0[0], y0[1], y1[0], y1[1]}, tf, accc);
    }
    const f32x4 bb = lds_ld<f32x4>(lds + OFF_AUX + 512 + w * 64 + q * 16);
    const f32x4 cv = (accb - accc) * kNegLog2e + bb;
    lds_st<f32x4>(lds + OFF_TC + r * 256 + w * 64 + q * 16, cv);
    asm volatile("" ::: "memory");
    lds_st<unsigned char>(lds + OFF_CNT + w, (unsigned char)1);
    asm volatile("" ::: "memory");
  }
  u32x4 wb[4][2];
  {
    LDS_AS char* pa = lds + lane * 16;
    f32x4 a[3][2], d[3][2];
    auto ld = [&](int k) {
      const int o_ = (k >> 1) * kWStride + (k & 1) * 2048;
      a[k % 3][0] = lds_ld<f32x4>(pa + OFF_WAC + o_);
      d[k % 3][0] = lds_ld<f32x4>(pa + OFF_WD + o_);
      a[k % 3][1] = lds_ld<f32x4>(pa + OFF_WAC + o_ + 1024);
      d[k % 3][1] = lds_ld<f32x4>(pa + OFF_WD + o_ + 1024);
    };
    ld(0);
    ld(1);
#pragma unroll
    for (int k = 0; k < 8; ++k) {
      if (k + 2 < 8) ld(k + 2);
      __builtin_amdgcn_sched_barrier(0);
      const f32x4 v0 = __builtin_elementwise_fma(d[k % 3][0], tq[k & 1][0], a[k % 3][0]);
      const f32x4 v1 = __builtin_elementwise_fma(d[k % 3][1], tq[k & 1][1], a[k % 3][1]);
      wb[k >> 1][k & 1] = u32x4{pk_bf16(lo2(v0)), pk_bf16(hi2(v0)), pk_bf16(lo2(v1)), pk_bf16(hi2(v1))};
      asm volatile("" : "+v"(wb[k >> 1][k & 1]));
      __builtin_amdgcn_sched_barrier(0);
    }
  }
  f32x4 c[4];
  for (;;) {
    const unsigned cnt = lds_ldv<unsigned>(lds + OFF_CNT);
#pragma unroll
    for (int ht = 0; ht < 4; ++ht)
#pragma unroll
      for (int i = 0; i < 4; ++i) c[ht][i] = lds_ldv<float>(tc + ht * 64 + r * 4);
    if (__builtin_amdgcn_readfirstlane(cnt) == 0x01010101u) break;
  }
  f32x2 w2[4];
#pragma unroll
  for (int ht = 0; ht < 4; ++ht) w2[ht] = f32x2{lds_ldv<float>(lds + OFF_AUX + ht * 64 + r * 4), lds_ldv<float>(lds + OFF_AUX + ht * 64 + r * 4)};
  const f32x2 init = lds_ld<f32x2>(lds + OFF_AUX + 256);
  STAMP(6);

  LDS_AS char* hl = lds + OFF_HIST + w * kHistB;
  LDS_AS char* lm = lds + OFF_LM + w * 256 + q * 8;
  const int rrow = ((r & 2) << 2) | ((r >> 2) << 1) | (r & 1);
  int roffA = rrow * kRowB + ((q ^ (rrow & 2) ^ ((rrow >> 1) & 4)) * 16);
  int roffB = roffA + 2048;
  const int trow = ((lane & 8)) | (q << 1) | ((lane >> 2) & 1);
  int toffA = trow * kRowB + ((((lane >> 1) & 1) ^ (trow & 2) ^ ((trow >> 1) & 4)) * 16) + (lane & 1) * 8;
  int toffB = toffA + 2048;
  f32x4 o[4];
#pragma unroll
  for (int dt = 0; dt < 4; ++dt) o[dt] = f32x4{0, 0, 0, 0};

  int rem = n;
  asm volatile("s_waitcnt vmcnt(2)" : "+v"(pf0)::"memory");
  STAMP(7);
#pragma unroll 1
  for (;;) {
    u32x4 sc = {0, 0, 0, 0};
    {
      const u32x4 h0 = lds_ld<u32x4>(hl + roffA), h1 = lds_ld<u32x4>(hl + (roffA ^ 64));
      const f32x2 m0 = lds_ld<f32x2>(lm), m1 = lds_ld<f32x2>(lm + 32);
      f32x4 y[4];
#pragma unroll
      for (int ht = 0; ht < 4; ++ht) y[ht] = mfma32(h1, wb[ht][1], mfma32(h0, wb[ht][0], c[ht]));
      sc[0] = pk_bf16(sig_pair(lo2(y[2]), lo2(y[3]), w2[2], w2[3], sig_pair(lo2(y[0]), lo2(y[1]), w2[0], w2[1], init)) * m0);
      if (rem > 8) sc[1] = pk_bf16(sig_pair(hi2(y[2]), hi2(y[3]), w2[2], w2[3], sig_pair(hi2(y[0]), hi2(y[1]), w2[0], w2[1], init)) * m1);
    }
    STAMP(8);
    asm volatile("s_waitcnt vmcnt(0)" : "+v"(sc)::"memory");
    STAMP(9);
    if (rem > 16) {
      const u32x4 h0 = lds_ld<u32x4>(hl + roffB), h1 = lds_ld<u32x4>(hl + (roffB ^ 64));
      const f32x2 m0 = lds_ld<f32x2>(lm + 64), m1 = lds_ld<f32x2>(lm + 96);
      f32x4 y[4];
#pragma unroll
      for (int ht = 0; ht < 4; ++ht) y[ht] = mfma32(h1, wb[ht][1], mfma32(h0, wb[ht][0], c[ht]));
      sc[2] = pk_bf16(sig_pair(lo2(y[2]), lo2(y[3]), w2[2], w2[3], sig_pair(lo2(y[0]), lo2(y[1]), w2[0], w2[1], init)) * m0);
      if (rem > 24) sc[3] = pk_bf16(sig_pair(hi2(y[2]), hi2(y[3]), w2[2], w2[3], sig_pair(hi2(y[0]), hi2(y[1]), w2[0], w2[1], init)) * m1);
    }
#pragma unroll
    for (int dt = 0; dt < 4; ++dt) {
      const u32x2 x0 = tr_read(hl + (toffA ^ (32 * dt))), x1 = tr_read(hl + (toffB ^ (32 * dt)));
      o[dt] = mfma32(sc, u32x4{x0[0], x0[1], x1[0], x1[1]}, o[dt]);
    }
    rem -= 32;
    if (rem <= 0) break;
    roffA += 4096;
    toffA += 4096;
    roffB = 6144 + (r & 1) * kRowB + q * 16;
    toffB = 6144 + ((lane >> 2) & 1) * kRowB + (lane & 3) * 8;
    lm += 128;
  }
  STAMP(10);
  float s4[4];
#pragma unroll
  for (int dt = 0; dt < 4; ++dt) { const f32x2 t = lo2(o[dt]) + hi2(o[dt]); s4[dt] = t[0] + t[1]; }
  const auto x02 = __builtin_amdgcn_permlane32_swap(__float_as_uint(s4[0]), __float_as_uint(s4[2]), false, false);
  const auto x13 = __builtin_amdgcn_permlane32_swap(__float_as_uint(s4[1]), __float_as_uint(s4[3]), false, false);
  const float u0 = __uint_as_float(x02[0]) + __uint_as_float(x02[1]);
  const float u1 = __uint_as_float(x13[0]) + __uint_as_float(x13[1]);
  const auto y01 = __builtin_amdgcn_permlane16_swap(__float_as_uint(u0), __float_as_uint(u1), false, false);
  const float res = (__uint_as_float(y01[0]) + __uint_as_float(y01[1])) * kDelta;
  outb[lane] = (unsigned short)pk_bf16(f32x2{res, res});
  STAMP_FLUSH(b);
}

extern "C" void kernel_launch(void* const* d_in, const int* in_sizes, int n_in, void* d_out, int out_size, void* d_ws, size_t ws_size,
                              hipStream_t stream) {
  const int B = in_sizes[0] / 64;
  din_kernel<<<B / kWaves, kWaves * 64, 0, stream>>>((const unsigned short*)d_in[0], (const unsigned short*)d_in[1], (const unsigned short*)d_in[2],
                                                     (const unsigned short*)d_in[3], (const unsigned short*)d_in[4], (const unsigned short*)d_in[5],
                                                     (const unsigned short*)d_in[6], (unsigned short*)d_out DBG_PASS);
}
```

```cpp
#include <hip/hip_runtime.h>
#include <hip/hip_bf16.h>

#define STAMP(i)
#define STAMP_DECL
#define STAMP_FLUSH(wv)
#define DBG_ARG
#define DBG_PASS
#define B_EARLY 0

typedef float f32x4 __attribute__((ext_vector_type(4)));
typedef float f32x2 __attribute__((ext_vector_type(2)));
typedef short s16x4 __attribute__((ext_vector_type(4)));
typedef __bf16 bf16x8 __attribute__((ext_vector_type(8)));
typedef __bf16 bf16x2 __attribute__((ext_vector_type(2)));
typedef unsigned u32x4 __attribute__((ext_vector_type(4)));
typedef unsigned u32x2 __attribute__((ext_vector_type(2)));
#define LDS_AS __attribute__((address_space(3)))
#define DI __device__ __forceinline__

constexpr int kL = 50;
constexpr int kWaves = 8;
constexpr int kSlots = 16;
constexpr int kRowB = 128;
constexpr int kHistB = kL * kRowB;
constexpr int OFF_WAC = 0;
constexpr int OFF_WD = OFF_WAC + 16384;
constexpr int OFF_WBC = OFF_WD + 16384;
constexpr int OFF_TC = OFF_WBC + 16384;
constexpr int OFF_LM = OFF_TC + kSlots * 256;
constexpr int OFF_TB = OFF_LM + kSlots * 256;
constexpr int OFF_AUX = OFF_TB + kSlots * 144;
constexpr int OFF_CNT = OFF_AUX + 768;
constexpr int OFF_HIST = ((OFF_CNT + 256) + 255) / 256 * 256;
constexpr int LDS_BYTES = OFF_HIST + kSlots * kHistB;
static_assert(LDS_BYTES <= 163840, "lds");

constexpr float kNegLog2e = -1.4426950408889634f;
constexpr float kShift = 60.0f;
constexpr float kDelta = 8.673617379884035e-19f;
constexpr float kB2Scale = 72057594037927936.0f;

DI float lo2f(unsigned u) { return __uint_as_float(u << 16); }
DI float hi2f(unsigned u) { return __uint_as_float(u & 0xffff0000u); }
DI unsigned pk_bf16(f32x2 v) { return __builtin_bit_cast(unsigned, __builtin_convertvector(v, bf16x2)); }
DI f32x4 mfma32(u32x4 a, u32x4 b, f32x4 c) {
  return __builtin_amdgcn_mfma_f32_16x16x32_bf16(__builtin_bit_cast(bf16x8, a), __builtin_bit_cast(bf16x8, b), c, 0, 0, 0);
}
DI u32x2 tr_read(LDS_AS char* p) {
  return __builtin_bit_cast(u32x2, __builtin_amdgcn_ds_read_tr16_b64_v4i16((LDS_AS s16x4*)p));
}
template <typename T> DI T lds_ld(LDS_AS char* p) { return *(LDS_AS T*)p; }
template <typename T> DI T lds_ldv(LDS_AS char* p) { return *(volatile LDS_AS T*)p; }
template <typename T> DI void lds_st(LDS_AS char* p, T v) { *(LDS_AS T*)p = v; }
DI void dma16(unsigned ldsaddr, unsigned voff, __amdgpu_buffer_rsrc_t srd) {
  asm volatile("s_mov_b32 m0, %0\n\ts_nop 0\n\tbuffer_load_dwordx4 %1, %2, 0 offen nt lds" ::"s"(ldsaddr), "v"(voff), "s"(srd) : "memory");
}
DI f32x2 pkfma(f32x2 a, f32x2 b, f32x2 c) { return __builtin_elementwise_fma(a, b, c); }
DI float expc(float y) { return __builtin_amdgcn_fmed3f(__builtin_amdgcn_exp2f(y), 0.f, 1.f); }
DI f32x2 lo2(f32x4 v) { return f32x2{v[0], v[1]}; }
DI f32x2 hi2(f32x4 v) { return f32x2{v[2], v[3]}; }

DI f32x2 sig_pair(f32x2 ya, f32x2 yb, f32x2 wa, f32x2 wb, f32x2 acc) {
  const f32x2 dl = {kDelta, kDelta};
  const f32x2 da = f32x2{expc(ya[0]), expc(ya[1])} + dl;
  const f32x2 db = f32x2{expc(yb[0]), expc(yb[1])} + dl;
  const f32x2 den = da * db;
  const f32x2 num = pkfma(wa, db, wb * da);
  const f32x2 rc = {__builtin_amdgcn_rcpf(den[0]), __builtin_amdgcn_rcpf(den[1])};
  return pkfma(num, rc, acc);
}

DI int compact(unsigned m32, LDS_AS char* tc, int lane) {
  const unsigned long long M = __builtin_amdgcn_ballot_w64(__uint_as_float(m32) != 0.f) & ((1ull << kL) - 1);
  const bool act = __builtin_amdgcn_inverse_ballot_w64(M);
  int n = __popcll(M);
  asm volatile("" : "+s"(n));
  const unsigned rk = __builtin_amdgcn_mbcnt_hi((unsigned)(M >> 32), __builtin_amdgcn_mbcnt_lo((unsigned)M, 0));
  const unsigned pos = act ? rk : (unsigned)(n + lane) - rk;
  lds_st<unsigned>(tc + pos * 4, act ? (unsigned)(lane * kRowB) : 0x7f00u);
  lds_st<unsigned>(tc + (OFF_LM - OFF_TC) + pos * 4, act ? m32 : 0u);
  return n;
}

template <int W0, int W1, int S0>
DI float run_batch(LDS_AS char* hl, LDS_AS char* lm, int n, const u32x4 (&wb)[4][2], const f32x4 (&c)[4], const f32x2 (&w2)[4], f32x2 init,
                   int roffA, int toffA, int lane
                   ) {
  const int q = lane >> 4, r = lane & 15;
  int roffB = roffA + 2048, toffB = toffA + 2048;
  f32x4 o[4];
#pragma unroll
  for (int dt = 0; dt < 4; ++dt) o[dt] = f32x4{0, 0, 0, 0};
  int rem = n;
  asm volatile("s_waitcnt vmcnt(%0)" ::"i"(W0) : "memory");
  STAMP(S0);
#pragma unroll 1
  for (;;) {
    u32x4 sc = {0, 0, 0, 0};
    {
      const u32x4 h0 = lds_ld<u32x4>(hl + roffA), h1 = lds_ld<u32x4>(hl + (roffA ^ 64));
      const f32x2 m0 = lds_ld<f32x2>(lm), m1 = lds_ld<f32x2>(lm + 32);
      f32x4 y[4];
#pragma unroll
      for (int ht = 0; ht < 4; ++ht) y[ht] = mfma32(h1, wb[ht][1], mfma32(h0, wb[ht][0], c[ht]));
      sc[0] = pk_bf16(sig_pair(lo2(y[2]), lo2(y[3]), w2[2], w2[3], sig_pair(lo2(y[0]), lo2(y[1]), w2[0], w2[1], init)) * m0);
      if (rem > 8) sc[1] = pk_bf16(sig_pair(hi2(y[2]), hi2(y[3]), w2[2], w2[3], sig_pair(hi2(y[0]), hi2(y[1]), w2[0], w2[1], init)) * m1);
    }
    asm volatile("s_waitcnt vmcnt(%1)" : "+v"(sc) : "i"(W1) : "memory");
    STAMP(S0 + 1);
    if (rem > 16) {
      const u32x4 h0 = lds_ld<u32x4>(hl + roffB), h1 = lds_ld<u32x4>(hl + (roffB ^ 64));
      const f32x2 m0 = lds_ld<f32x2>(lm + 64), m1 = lds_ld<f32x2>(lm + 96);
      f32x4 y[4];
#pragma unroll
      for (int ht = 0; ht < 4; ++ht) y[ht] = mfma32(h1, wb[ht][1], mfma32(h0, wb[ht][0], c[ht]));
      sc[2] = pk_bf16(sig_pair(lo2(y[2]), lo2(y[3]), w2[2], w2[3], sig_pair(lo2(y[0]), lo2(y[1]), w2[0], w2[1], init)) * m0);
      if (rem > 24) sc[3] = pk_bf16(sig_pair(hi2(y[2]), hi2(y[3]), w2[2], w2[3], sig_pair(hi2(y[0]), hi2(y[1]), w2[0], w2[1], init)) * m1);
    }
#pragma unroll
    for (int dt = 0; dt < 4; ++dt) {
      const u32x2 x0 = tr_read(hl + (toffA ^ (32 * dt))), x1 = tr_read(hl + (toffB ^ (32 * dt)));
      o[dt] = mfma32(sc, u32x4{x0[0], x0[1], x1[0], x1[1]}, o[dt]);
    }
    rem -= 32;
    if (rem <= 0) break;
    roffA += 4096;
    toffA += 4096;
    roffB = 6144 + (r & 1) * kRowB + q * 16;
    toffB = 6144 + ((lane >> 2) & 1) * kRowB + (lane & 3) * 8;
    lm += 128;
  }
  float s4[4];
#pragma unroll
  for (int dt = 0; dt < 4; ++dt) { const f32x2 t = lo2(o[dt]) + hi2(o[dt]); s4[dt] = t[0] + t[1]; }
  const auto x02 = __builtin_amdgcn_permlane32_swap(__float_as_uint(s4[0]), __float_as_uint(s4[2]), false, false);
  const auto x13 = __builtin_amdgcn_permlane32_swap(__float_as_uint(s4[1]), __float_as_uint(s4[3]), false, false);
  const float u0 = __uint_as_float(x02[0]) + __uint_as_float(x02[1]);
  const float u1 = __uint_as_float(x13[0]) + __uint_as_float(x13[1]);
  const auto y01 = __builtin_amdgcn_permlane16_swap(__float_as_uint(u0), __float_as_uint(u1), false, false);
  return (__uint_as_float(y01[0]) + __uint_as_float(y01[1])) * kDelta;
}
#define ST_PASS

__global__ __launch_bounds__(512) void din_kernel(const unsigned short* __restrict__ target, const unsigned short* __restrict__ hist,
                                                  const unsigned short* __restrict__ mask, const unsigned short* __restrict__ w1,
                                                  const unsigned short* __restrict__ b1, const unsigned short* __restrict__ w2g,
                                                  const unsigned short* __restrict__ b2, unsigned short* __restrict__ out DBG_ARG) {
  STAMP_DECL
  STAMP(0);
  __shared__ __attribute__((aligned(256))) char lds_raw[LDS_BYTES];
  LDS_AS char* lds = (LDS_AS char*)lds_raw;
  const int tid = threadIdx.x, lane = tid & 63;
  const int w = __builtin_amdgcn_readfirstlane(tid >> 6);
  const int b = blockIdx.x * kSlots + 2 * w;
  const int q = lane >> 4, r = lane & 15;
  const unsigned ldsbase = (unsigned)(size_t)lds;

  asm volatile("" ::"s"(target), "s"(hist), "s"(mask), "s"(w1), "s"(b1), "s"(w2g), "s"(b2), "s"(out));
  STAMP(20);
  unsigned mA = 0, mB = 0, t32, aux = 0;
  u32x4 wa, wc, wd;
  const unsigned short* histb = hist + (size_t)b * (kL * 64);
  {
    const unsigned moff = (unsigned)(min(lane, kL - 1) * 2);
    const unsigned short* auxp = w == 4 ? w2g : w == 5 ? b2 : b1;
    const unsigned aoff = w == 5 ? 0u : (unsigned)lane * 2;
    asm volatile("s_nop 4\n\tglobal_load_short_d16_hi %0, %4, %5 nt\n\tglobal_load_short_d16_hi %1, %4, %5 offset:100 nt\n\t"
                 "global_load_dword %2, %6, %7 nt\n\tglobal_load_short_d16_hi %3, %8, %9"
                 : "+v"(mA), "+v"(mB), "=&v"(t32), "+v"(aux)
                 : "v"(moff), "s"(mask + (size_t)b * kL), "v"(lane * 4), "s"(target + (size_t)b * 64), "v"(aoff), "s"(auxp));
    const unsigned woff = (unsigned)((tid >> 5) * 512 + (tid & 31) * 4);
    asm volatile("global_load_dword %0, %12, %13\n\tglobal_load_dword %1, %12, %13 offset:128\n\tglobal_load_dword %2, %12, %13 offset:256\n\tglobal_load_dword %3, %12, %13 offset:384\n\t"
                 "global_load_dword %4, %12, %14\n\tglobal_load_dword %5, %12, %14 offset:128\n\tglobal_load_dword %6, %12, %14 offset:256\n\tglobal_load_dword %7, %12, %14 offset:384\n\t"
                 "global_load_dword %8, %12, %15\n\tglobal_load_dword %9, %12, %15 offset:128\n\tglobal_load_dword %10, %12, %15 offset:256\n\tglobal_load_dword %11, %12, %15 offset:384"
                 : "=&v"(wa[0]), "=&v"(wa[1]), "=&v"(wa[2]), "=&v"(wa[3]), "=&v"(wc[0]), "=&v"(wc[1]), "=&v"(wc[2]), "=&v"(wc[3]),
                   "=&v"(wd[0]), "=&v"(wd[1]), "=&v"(wd[2]), "=&v"(wd[3])
                 : "v"(woff), "s"(w1), "s"(w1 + 8192), "s"(w1 + 12288));
    asm volatile("s_mov_b32 m0, %0\n\ts_nop 0\n\tglobal_load_lds_dwordx4 %1, %2\n\tglobal_load_lds_dwordx4 %1, %2 offset:1024"
                 ::"s"(ldsbase + OFF_WBC + w * 2048), "v"(lane * 16), "s"(w1 + 4096 + w * 1024) : "memory");
  }
  const unsigned histl = ldsbase + OFF_HIST + w * (2 * kHistB);
  const __amdgpu_buffer_rsrc_t srdA = __builtin_amdgcn_make_buffer_rsrc((void*)histb, (short)0, kHistB, 0x00020000);
  const __amdgpu_buffer_rsrc_t srdB = __builtin_amdgcn_make_buffer_rsrc((void*)(histb + kL * 64), (short)0, kHistB, 0x00020000);
  LDS_AS char* tc = lds + OFF_TC + w * 512;
  const unsigned chunk0 = (unsigned)(((lane & 7) ^ ((lane >> 3) & 2)) * 16), chunk1 = chunk0 ^ 64u;
  STAMP(1);

  asm volatile("s_waitcnt vmcnt(16)" : "+v"(mA), "+v"(mB)::"memory");
  STAMP(2);
  const int nA = compact(mA, tc, lane);
  const int nB = compact(mB, tc + 256, lane);
  __builtin_amdgcn_wave_barrier();
  asm volatile("" ::: "memory");
  LDS_AS char* lo_ = tc + (lane >> 3) * 4;
  u32x4 eB;
  {
    const unsigned e0 = lds_ld<unsigned>(lo_), e1 = lds_ld<unsigned>(lo_ + 32), e2 = lds_ld<unsigned>(lo_ + 64), e3 = lds_ld<unsigned>(lo_ + 96);
    eB = u32x4{lds_ld<unsigned>(lo_ + 256), lds_ld<unsigned>(lo_ + 288), lds_ld<unsigned>(lo_ + 320), lds_ld<unsigned>(lo_ + 352)};
    dma16(histl, e0 | chunk0, srdA);
    dma16(histl + 1024, e1 | chunk1, srdA);
    dma16(histl + 2048, e2 | chunk0, srdA);
    dma16(histl + 3072, e3 | chunk1, srdA);
    eB = eB | u32x4{chunk0, chunk1, chunk0, chunk1};
  }
  auto rare = [&](LDS_AS char* l, unsigned dst, __amdgpu_buffer_rsrc_t srd) {
    const unsigned e4 = lds_ld<unsigned>(l + 128), e5 = lds_ld<unsigned>(l + 160), e6 = lds_ld<unsigned>(l + 192);
    dma16(dst + 4096, e4 | chunk0, srd);
    dma16(dst + 5120, e5 | chunk1, srd);
    if (lane < 16) dma16(dst + 6144, e6 | chunk0, srd);
  };
  auto dmaB = [&]() {
    dma16(histl + kHistB, eB[0], srdB);
    dma16(histl + kHistB + 1024, eB[1], srdB);
    dma16(histl + kHistB + 2048, eB[2], srdB);
    dma16(histl + kHistB + 3072, eB[3], srdB);
  };
  STAMP(3);
  asm volatile("s_waitcnt vmcnt(4)" : "+v"(t32), "+v"(aux), "+v"(wa), "+v"(wc), "+v"(wd)::"memory");
  STAMP(4);
  if (nA > 32) rare(lo_, histl, srdA);
  if (nB > 32) rare(lo_ + 256, histl + kHistB, srdB);
  asm volatile("" ::: "memory");
  {
    const f32x2 k2 = {kNegLog2e, kNegLog2e};
    lds_st<f32x2>(tc + lane * 8, f32x2{lo2f(t32), hi2f(t32)} * k2);
    lds_st<unsigned>(lds + OFF_TB + (2 * w + (lane >> 5)) * 144 + (lane & 31) * 4, t32);
    const int hp = lane & 31;
    const int off = (hp >> 3) * 4096 + ((w >> 2) * 2 + (lane >> 5)) * 1024 + ((w & 3) * 16 + (hp & 7)) * 16;
    const f32x4 s0 = (f32x4{lo2f(wa[0]), lo2f(wa[1]), lo2f(wa[2]), lo2f(wa[3])} + f32x4{lo2f(wc[0]), lo2f(wc[1]), lo2f(wc[2]), lo2f(wc[3])}) * kNegLog2e;
    const f32x4 s1 = (f32x4{hi2f(wa[0]), hi2f(wa[1]), hi2f(wa[2]), hi2f(wa[3])} + f32x4{hi2f(wc[0]), hi2f(wc[1]), hi2f(wc[2]), hi2f(wc[3])}) * kNegLog2e;
    lds_st<f32x4>(lds + OFF_WAC + off, s0);
    lds_st<f32x4>(lds + OFF_WAC + off + 128, s1);
    lds_st<f32x4>(lds + OFF_WD + off, f32x4{lo2f(wd[0]), lo2f(wd[1]), lo2f(wd[2]), lo2f(wd[3])});
    lds_st<f32x4>(lds + OFF_WD + off + 128, f32x4{hi2f(wd[0]), hi2f(wd[1]), hi2f(wd[2]), hi2f(wd[3])});
  }
  if (w >= 4) {
    const float sa = w == 4 ? 1.f : w == 5 ? kB2Scale : kNegLog2e, sb = w == 6 ? -kShift : 0.f;
    const float v = __builtin_fmaf(__uint_as_float(aux), sa, sb);
    lds_st<float>(lds + OFF_AUX + (w & 3) * 256 + lane * 4, w == 7 ? 0.f : v);
  }
  __builtin_amdgcn_wave_barrier();
  asm volatile("" ::: "memory");
  f32x4 tA[2][2], tB[2][2];
#pragma unroll
  for (int s = 0; s < 2; ++s)
#pragma unroll
    for (int jh = 0; jh < 2; ++jh) {
      tA[s][jh] = lds_ld<f32x4>(tc + (32 * s + 8 * q + 4 * jh) * 4);
      tB[s][jh] = lds_ld<f32x4>(tc + 256 + (32 * s + 8 * q + 4 * jh) * 4);
    }
  asm volatile("s_waitcnt lgkmcnt(0)\n\ts_barrier"
               : "+v"(tA[0][0]), "+v"(tA[0][1]), "+v"(tA[1][0]), "+v"(tA[1][1]), "+v"(tB[0][0]), "+v"(tB[0][1]), "+v"(tB[1][0]), "+v"(tB[1][1])::"memory");
  STAMP(5);
  dmaB();
  STAMP(6);

  if (w < 4) {
    LDS_AS char* base = lds + OFF_WBC + (8 * q + (r >> 2)) * 128 + w * 32 + (lane & 3) * 8;
    f32x4 accb = {0, 0, 0, 0}, accc = {0, 0, 0, 0};
    const f32x4 bb = lds_ld<f32x4>(lds + OFF_AUX + 512 + w * 64 + q * 16);
#pragma unroll
    for (int s = 0; s < 2; ++s) {
      const u32x2 x0 = tr_read(base + s * 4096), x1 = tr_read(base + s * 4096 + 512);
      const u32x2 y0 = tr_read(base + 8192 + s * 4096), y1 = tr_read(base + 8192 + s * 4096 + 512);
      const u32x4 tf = lds_ld<u32x4>(lds + OFF_TB + r * 144 + s * 64 + q * 16);
      accb = mfma32(u32x4{x0[0], x0[1], x1[0], x1[1]}, tf, accb);
      accc = mfma32(u32x4{y0[0], y0[1], y1[0], y1[1]}, tf, accc);
    }
    const f32x4 cv = (accb - accc) * kNegLog2e + bb;
    lds_st<f32x4>(lds + OFF_TC + r * 256 + w * 64 + q * 16, cv);
    asm volatile("" ::: "memory");
    lds_st<unsigned char>(lds + OFF_CNT + w, (unsigned char)1);
    asm volatile("" ::: "memory");
  }
  STAMP(7);
  u32x4 wbA[4][2], wbB[4][2];
  {
    LDS_AS char* pa = lds + lane * 16;
    f32x4 a[3][2], d[3][2];
    auto ld = [&](int k) {
      a[k % 3][0] = lds_ld<f32x4>(pa + OFF_WAC + k * 2048);
      d[k % 3][0] = lds_ld<f32x4>(pa + OFF_WD + k * 2048);
      a[k % 3][1] = lds_ld<f32x4>(pa + OFF_WAC + k * 2048 + 1024);
      d[k % 3][1] = lds_ld<f32x4>(pa + OFF_WD + k * 2048 + 1024);
    };
    ld(0);
    ld(1);
#pragma unroll
    for (int k = 0; k < 8; ++k) {
      if (k + 2 < 8) ld(k + 2);
      __builtin_amdgcn_sched_barrier(0);
      {
        const f32x4 v0 = __builtin_elementwise_fma(d[k % 3][0], tA[k & 1][0], a[k % 3][0]);
        const f32x4 v1 = __builtin_elementwise_fma(d[k % 3][1], tA[k & 1][1], a[k % 3][1]);
        wbA[k >> 1][k & 1] = u32x4{pk_bf16(lo2(v0)), pk_bf16(hi2(v0)), pk_bf16(lo2(v1)), pk_bf16(hi2(v1))};
        const f32x4 u0 = __builtin_elementwise_fma(d[k % 3][0], tB[k & 1][0], a[k % 3][0]);
        const f32x4 u1 = __builtin_elementwise_fma(d[k % 3][1], tB[k & 1][1], a[k % 3][1]);
        wbB[k >> 1][k & 1] = u32x4{pk_bf16(lo2(u0)), pk_bf16(hi2(u0)), pk_bf16(lo2(u1)), pk_bf16(hi2(u1))};
      }
      asm volatile("" : "+v"(wbA[k >> 1][k & 1]), "+v"(wbB[k >> 1][k & 1]));
      __builtin_amdgcn_sched_barrier(0);
    }
  }
  const int hoff = (2 * (r & 7) + (r >> 3)) * 4;
  f32x4 cA[4], cB[4];
  for (;;) {
    const unsigned cnt = lds_ldv<unsigned>(lds + OFF_CNT);
#pragma unroll
    for (int ht = 0; ht < 4; ++ht)
#pragma unroll
      for (int i = 0; i < 4; ++i) cA[ht][i] = lds_ldv<float>(tc + ht * 64 + hoff);
    if (__builtin_amdgcn_readfirstlane(cnt) == 0x01010101u) break;
  }
#pragma unroll
  for (int ht = 0; ht < 4; ++ht)
#pragma unroll
    for (int i = 0; i < 4; ++i) cB[ht][i] = lds_ldv<float>(tc + 256 + ht * 64 + hoff);
  f32x2 w2[4];
#pragma unroll
  for (int ht = 0; ht < 4; ++ht) w2[ht] = f32x2{lds_ldv<float>(lds + OFF_AUX + ht * 64 + hoff), lds_ldv<float>(lds + OFF_AUX + ht * 64 + hoff)};
  const f32x2 init = lds_ld<f32x2>(lds + OFF_AUX + 256);

  LDS_AS char* hl = lds + OFF_HIST + w * (2 * kHistB);
  LDS_AS char* lm = lds + OFF_LM + w * 512 + q * 8;
  const int rrow = ((r & 2) << 2) | ((r >> 2) << 1) | (r & 1);
  int roffA = rrow * kRowB + ((q ^ (rrow & 2) ^ ((rrow >> 1) & 4)) * 16);
  const int trow = ((lane & 8)) | (q << 1) | ((lane >> 2) & 1);
  int toffA = trow * kRowB + ((((lane >> 1) & 1) ^ (trow & 2) ^ ((trow >> 1) & 4)) * 16) + (lane & 1) * 8;
  asm volatile("" : "+v"(roffA), "+v"(toffA));
  STAMP(8);
  const float resA = run_batch<6, 4, 9>(hl, lm, nA, wbA, cA, w2, init, roffA, toffA, lane ST_PASS);
  const float resB = run_batch<2, 0, 11>(hl + kHistB, lm + 256, nB, wbB, cB, w2, init, roffA, toffA, lane ST_PASS);
  const unsigned pk = pk_bf16(f32x2{resA, resB});
  unsigned short* outb = out + (size_t)b * 64;
  outb[lane] = (unsigned short)pk;
  outb[64 + lane] = (unsigned short)(pk >> 16);
  STAMP_FLUSH(blockIdx.x * kWaves + w);
}

extern "C" void kernel_launch(void* const* d_in, const int* in_sizes, int n_in, void* d_out, int out_size, void* d_ws, size_t ws_size,
                              hipStream_t stream) {
  const int B = in_sizes[0] / 64;
  din_kernel<<<B / kSlots, kWaves * 64, 0, stream>>>((const unsigned short*)d_in[0], (const unsigned short*)d_in[1], (const unsigned short*)d_in[2],
                                                     (const unsigned short*)d_in[3], (const unsigned short*)d_in[4], (const unsigned short*)d_in[5],
                                                     (const unsigned short*)d_in[6], (unsigned short*)d_out DBG_PASS);
}
```

```cpp
#include <hip/hip_runtime.h>
#include <hip/hip_bf16.h>

#define DIAG_A 0
#define DIAG_B 0
#define DIAG_W -1

#define NSTAMP 14
#define STAMP(i) do{ if ((i)==DIAG_A || (i)==DIAG_B) { unsigned long long _a; __builtin_amdgcn_sched_barrier(0); asm volatile("s_memrealtime %0\n\ts_waitcnt lgkmcnt(0)":"=s"(_a)::"memory"); __builtin_amdgcn_sched_barrier(0); st_[(i)==DIAG_A?0:1][0]=_a; } }while(0)
#define STAMP_DECL unsigned long long st_[NSTAMP][2];
#define STAMP_FLUSH(wv) do{ asm volatile("s_waitcnt vmcnt(0)":::"memory"); STAMP(NSTAMP-1); \
   if (DIAG_W < 0 || (int)((wv)&7)==DIAG_W) { unsigned long long dl=(st_[1][0]-st_[0][0])*10+1500; if(dl>20000) dl=20000; while(__builtin_amdgcn_s_memrealtime()-t00_ < dl) __builtin_amdgcn_s_sleep(2); } }while(0)
#define DBG_ARG
#define DBG_PASS
#define DIN_STAMPS
#define B_EARLY 0

typedef float f32x4 __attribute__((ext_vector_type(4)));
typedef float f32x2 __attribute__((ext_vector_type(2)));
typedef short s16x4 __attribute__((ext_vector_type(4)));
typedef __bf16 bf16x8 __attribute__((ext_vector_type(8)));
typedef __bf16 bf16x2 __attribute__((ext_vector_type(2)));
typedef unsigned u32x4 __attribute__((ext_vector_type(4)));
typedef unsigned u32x2 __attribute__((ext_vector_type(2)));
#define LDS_AS __attribute__((address_space(3)))
#define DI __device__ __forceinline__

constexpr int kL = 50;
constexpr int kWaves = 8;
constexpr int kSlots = 16;
constexpr int kRowB = 128;
constexpr int kHistB = kL * kRowB;
constexpr int OFF_WAC = 0;
constexpr int OFF_WD = OFF_WAC + 16384;
constexpr int OFF_WBC = OFF_WD + 16384;
constexpr int OFF_TC = OFF_WBC + 16384;
constexpr int OFF_LM = OFF_TC + kSlots * 256;
constexpr int OFF_TB = OFF_LM + kSlots * 256;
constexpr int OFF_AUX = OFF_TB + kSlots * 144;
constexpr int OFF_CNT = OFF_AUX + 768;
constexpr int OFF_HIST = ((OFF_CNT + 256) + 255) / 256 * 256;
constexpr int LDS_BYTES = OFF_HIST + kSlots * kHistB;
static_assert(LDS_BYTES <= 163840, "lds");

constexpr float kNegLog2e = -1.4426950408889634f;
constexpr float kShift = 60.0f;
constexpr float kDelta = 8.673617379884035e-19f;
constexpr float kB2Scale = 72057594037927936.0f;

DI float lo2f(unsigned u) { return __uint_as_float(u << 16); }
DI float hi2f(unsigned u) { return __uint_as_float(u & 0xffff0000u); }
DI unsigned pk_bf16(f32x2 v) { return __builtin_bit_cast(unsigned, __builtin_convertvector(v, bf16x2)); }
DI f32x4 mfma32(u32x4 a, u32x4 b, f32x4 c) {
  return __builtin_amdgcn_mfma_f32_16x16x32_bf16(__builtin_bit_cast(bf16x8, a), __builtin_bit_cast(bf16x8, b), c, 0, 0, 0);
}
DI u32x2 tr_read(LDS_AS char* p) {
  return __builtin_bit_cast(u32x2, __builtin_amdgcn_ds_read_tr16_b64_v4i16((LDS_AS s16x4*)p));
}
template <typename T> DI T lds_ld(LDS_AS char* p) { return *(LDS_AS T*)p; }
template <typename T> DI T lds_ldv(LDS_AS char* p) { return *(volatile LDS_AS T*)p; }
template <typename T> DI void lds_st(LDS_AS char* p, T v) { *(LDS_AS T*)p = v; }
DI void dma16(unsigned ldsaddr, unsigned voff, __amdgpu_buffer_rsrc_t srd) {
  asm volatile("s_mov_b32 m0, %0\n\ts_nop 0\n\tbuffer_load_dwordx4 %1, %2, 0 offen nt lds" ::"s"(ldsaddr), "v"(voff), "s"(srd) : "memory");
}
DI f32x2 pkfma(f32x2 a, f32x2 b, f32x2 c) { return __builtin_elementwise_fma(a, b, c); }
DI float expc(float y) { return __builtin_amdgcn_fmed3f(__builtin_amdgcn_exp2f(y), 0.f, 1.f); }
DI f32x2 lo2(f32x4 v) { return f32x2{v[0], v[1]}; }
DI f32x2 hi2(f32x4 v) { return f32x2{v[2], v[3]}; }

DI f32x2 sig_pair(f32x2 ya, f32x2 yb, f32x2 wa, f32x2 wb, f32x2 acc) {
  const f32x2 dl = {kDelta, kDelta};
  const f32x2 da = f32x2{expc(ya[0]), expc(ya[1])} + dl;
  const f32x2 db = f32x2{expc(yb[0]), expc(yb[1])} + dl;
  const f32x2 den = da * db;
  const f32x2 num = pkfma(wa, db, wb * da);
  const f32x2 rc = {__builtin_amdgcn_rcpf(den[0]), __builtin_amdgcn_rcpf(den[1])};
  return pkfma(num, rc, acc);
}

DI int compact(unsigned m32, LDS_AS char* tc, int lane) {
  const unsigned long long M = __builtin_amdgcn_ballot_w64(__uint_as_float(m32) != 0.f) & ((1ull << kL) - 1);
  const bool act = __builtin_amdgcn_inverse_ballot_w64(M);
  int n = __popcll(M);
  asm volatile("" : "+s"(n));
  const unsigned rk = __builtin_amdgcn_mbcnt_hi((unsigned)(M >> 32), __builtin_amdgcn_mbcnt_lo((unsigned)M, 0));
  const unsigned pos = act ? rk : (unsigned)(n + lane) - rk;
  lds_st<unsigned>(tc + pos * 4, act ? (unsigned)(lane * kRowB) : 0x7f00u);
  lds_st<unsigned>(tc + (OFF_LM - OFF_TC) + pos * 4, act ? m32 : 0u);
  return n;
}

template <int W0, int W1, int S0>
DI float run_batch(LDS_AS char* hl, LDS_AS char* lm, int n, const u32x4 (&wb)[4][2], const f32x4 (&c)[4], const f32x2 (&w2)[4], f32x2 init,
                   int roffA, int toffA, int lane
                   , unsigned long long (&st_)[NSTAMP][2]
                   ) {
  const int q = lane >> 4, r = lane & 15;
  int roffB = roffA + 2048, toffB = toffA + 2048;
  f32x4 o[4];
#pragma unroll
  for (int dt = 0; dt < 4; ++dt) o[dt] = f32x4{0, 0, 0, 0};
  int rem = n;
  asm volatile("s_waitcnt vmcnt(%0)" ::"i"(W0) : "memory");
  STAMP(S0);
#pragma unroll 1
  for (;;) {
    u32x4 sc = {0, 0, 0, 0};
    {
      const u32x4 h0 = lds_ld<u32x4>(hl + roffA), h1 = lds_ld<u32x4>(hl + (roffA ^ 64));
      const f32x2 m0 = lds_ld<f32x2>(lm), m1 = lds_ld<f32x2>(lm + 32);
      f32x4 y[4];
#pragma unroll
      for (int ht = 0; ht < 4; ++ht) y[ht] = mfma32(h1, wb[ht][1], mfma32(h0, wb[ht][0], c[ht]));
      sc[0] = pk_bf16(sig_pair(lo2(y[2]), lo2(y[3]), w2[2], w2[3], sig_pair(lo2(y[0]), lo2(y[1]), w2[0], w2[1], init)) * m0);
      if (rem > 8) sc[1] = pk_bf16(sig_pair(hi2(y[2]), hi2(y[3]), w2[2], w2[3], sig_pair(hi2(y[0]), hi2(y[1]), w2[0], w2[1], init)) * m1);
    }
    asm volatile("s_waitcnt vmcnt(%1)" : "+v"(sc) : "i"(W1) : "memory");
    STAMP(S0 + 1);
    if (rem > 16) {
      const u32x4 h0 = lds_ld<u32x4>(hl + roffB), h1 = lds_ld<u32x4>(hl + (roffB ^ 64));
      const f32x2 m0 = lds_ld<f32x2>(lm + 64), m1 = lds_ld<f32x2>(lm + 96);
      f32x4 y[4];
#pragma unroll
      for (int ht = 0; ht < 4; ++ht) y[ht] = mfma32(h1, wb[ht][1], mfma32(h0, wb[ht][0], c[ht]));
      sc[2] = pk_bf16(sig_pair(lo2(y[2]), lo2(y[3]), w2[2], w2[3], sig_pair(lo2(y[0]), lo2(y[1]), w2[0], w2[1], init)) * m0);
      if (rem > 24) sc[3] = pk_bf16(sig_pair(hi2(y[2]), hi2(y[3]), w2[2], w2[3], sig_pair(hi2(y[0]), hi2(y[1]), w2[0], w2[1], init)) * m1);
    }
#pragma unroll
    for (int dt = 0; dt < 4; ++dt) {
      const u32x2 x0 = tr_read(hl + (toffA ^ (32 * dt))), x1 = tr_read(hl + (toffB ^ (32 * dt)));
      o[dt] = mfma32(sc, u32x4{x0[0], x0[1], x1[0], x1[1]}, o[dt]);
    }
    rem -= 32;
    if (rem <= 0) break;
    roffA += 4096;
    toffA += 4096;
    roffB = 6144 + (r & 1) * kRowB + q * 16;
    toffB = 6144 + ((lane >> 2) & 1) * kRowB + (lane & 3) * 8;
    lm += 128;
  }
  float s4[4];
#pragma unroll
  for (int dt = 0; dt < 4; ++dt) { const f32x2 t = lo2(o[dt]) + hi2(o[dt]); s4[dt] = t[0] + t[1]; }
  const auto x02 = __builtin_amdgcn_permlane32_swap(__float_as_uint(s4[0]), __float_as_uint(s4[2]), false, false);
  const auto x13 = __builtin_amdgcn_permlane32_swap(__float_as_uint(s4[1]), __float_as_uint(s4[3]), false, false);
  const float u0 = __uint_as_float(x02[0]) + __uint_as_float(x02[1]);
  const float u1 = __uint_as_float(x13[0]) + __uint_as_float(x13[1]);
  const auto y01 = __builtin_amdgcn_permlane16_swap(__float_as_uint(u0), __float_as_uint(u1), false, false);
  return (__uint_as_float(y01[0]) + __uint_as_float(y01[1])) * kDelta;
}
#define ST_PASS , st_

__global__ __launch_bounds__(512) void din_kernel(const unsigned short* __restrict__ target, const unsigned short* __restrict__ hist,
                                                  const unsigned short* __restrict__ mask, const unsigned short* __restrict__ w1,
                                                  const unsigned short* __restrict__ b1, const unsigned short* __restrict__ w2g,
                                                  const unsigned short* __restrict__ b2, unsigned short* __restrict__ out DBG_ARG) {
  STAMP_DECL
  unsigned long long t00_; asm volatile("s_memrealtime %0\n\ts_waitcnt lgkmcnt(0)":"=s"(t00_)::"memory"); st_[0][0]=t00_; st_[1][0]=t00_;
  __shared__ __attribute__((aligned(256))) char lds_raw[LDS_BYTES];
  LDS_AS char* lds = (LDS_AS char*)lds_raw;
  const int tid = threadIdx.x, lane = tid & 63;
  const int w = __builtin_amdgcn_readfirstlane(tid >> 6);
  const int b = blockIdx.x * kSlots + 2 * w;
  const int q = lane >> 4, r = lane & 15;
  const unsigned ldsbase = (unsigned)(size_t)lds;

  asm volatile("" ::"s"(target), "s"(hist), "s"(mask), "s"(w1), "s"(b1), "s"(w2g), "s"(b2), "s"(out));
  STAMP(20);
  unsigned mA = 0, mB = 0, t32, aux = 0;
  u32x4 wa, wc, wd;
  const unsigned short* histb = hist + (size_t)b * (kL * 64);
  {
    const unsigned moff = (unsigned)(min(lane, kL - 1) * 2);
    const unsigned short* auxp = w == 4 ? w2g : w == 5 ? b2 : b1;
    const unsigned aoff = w == 5 ? 0u : (unsigned)lane * 2;
    asm volatile("s_nop 4\n\tglobal_load_short_d16_hi %0, %4, %5 nt\n\tglobal_load_short_d16_hi %1, %4, %5 offset:100 nt\n\t"
                 "global_load_dword %2, %6, %7 nt\n\tglobal_load_short_d16_hi %3, %8, %9"
                 : "+v"(mA), "+v"(mB), "=&v"(t32), "+v"(aux)
                 : "v"(moff), "s"(mask + (size_t)b * kL), "v"(lane * 4), "s"(target + (size_t)b * 64), "v"(aoff), "s"(auxp));
    const unsigned woff = (unsigned)((tid >> 5) * 512 + (tid & 31) * 4);
    asm volatile("global_load_dword %0, %12, %13\n\tglobal_load_dword %1, %12, %13 offset:128\n\tglobal_load_dword %2, %12, %13 offset:256\n\tglobal_load_dword %3, %12, %13 offset:384\n\t"
                 "global_load_dword %4, %12, %14\n\tglobal_load_dword %5, %12, %14 offset:128\n\tglobal_load_dword %6, %12, %14 offset:256\n\tglobal_load_dword %7, %12, %14 offset:384\n\t"
                 "global_load_dword %8, %12, %15\n\tglobal_load_dword %9, %12, %15 offset:128\n\tglobal_load_dword %10, %12, %15 offset:256\n\tglobal_load_dword %11, %12, %15 offset:384"
                 : "=&v"(wa[0]), "=&v"(wa[1]), "=&v"(wa[2]), "=&v"(wa[3]), "=&v"(wc[0]), "=&v"(wc[1]), "=&v"(wc[2]), "=&v"(wc[3]),
                   "=&v"(wd[0]), "=&v"(wd[1]), "=&v"(wd[2]), "=&v"(wd[3])
                 : "v"(woff), "s"(w1), "s"(w1 + 8192), "s"(w1 + 12288));
    asm volatile("s_mov_b32 m0, %0\n\ts_nop 0\n\tglobal_load_lds_dwordx4 %1, %2\n\tglobal_load_lds_dwordx4 %1, %2 offset:1024"
                 ::"s"(ldsbase + OFF_WBC + w * 2048), "v"(lane * 16), "s"(w1 + 4096 + w * 1024) : "memory");
  }
  const unsigned histl = ldsbase + OFF_HIST + w * (2 * kHistB);
  const __amdgpu_buffer_rsrc_t srdA = __builtin_amdgcn_make_buffer_rsrc((void*)histb, (short)0, kHistB, 0x00020000);
  const __amdgpu_buffer_rsrc_t srdB = __builtin_amdgcn_make_buffer_rsrc((void*)(histb + kL * 64), (short)0, kHistB, 0x00020000);
  LDS_AS char* tc = lds + OFF_TC + w * 512;
  const unsigned chunk0 = (unsigned)(((lane & 7) ^ ((lane >> 3) & 2)) * 16), chunk1 = chunk0 ^ 64u;
  STAMP(1);

  asm volatile("s_waitcnt vmcnt(16)" : "+v"(mA), "+v"(mB)::"memory");
  STAMP(2);
  const int nA = compact(mA, tc, lane);
  const int nB = compact(mB, tc + 256, lane);
  __builtin_amdgcn_wave_barrier();
  asm volatile("" ::: "memory");
  LDS_AS char* lo_ = tc + (lane >> 3) * 4;
  u32x4 eB;
  {
    const unsigned e0 = lds_ld<unsigned>(lo_), e1 = lds_ld<unsigned>(lo_ + 32), e2 = lds_ld<unsigned>(lo_ + 64), e3 = lds_ld<unsigned>(lo_ + 96);
    eB = u32x4{lds_ld<unsigned>(lo_ + 256), lds_ld<unsigned>(lo_ + 288), lds_ld<unsigned>(lo_ + 320), lds_ld<unsigned>(lo_ + 352)};
    dma16(histl, e0 | chunk0, srdA);
    dma16(histl + 1024, e1 | chunk1, srdA);
    dma16(histl + 2048, e2 | chunk0, srdA);
    dma16(histl + 3072, e3 | chunk1, srdA);
    eB = eB | u32x4{chunk0, chunk1, chunk0, chunk1};
  }
  auto rare = [&](LDS_AS char* l, unsigned dst, __amdgpu_buffer_rsrc_t srd) {
    const unsigned e4 = lds_ld<unsigned>(l + 128), e5 = lds_ld<unsigned>(l + 160), e6 = lds_ld<unsigned>(l + 192);
    dma16(dst + 4096, e4 | chunk0, srd);
    dma16(dst + 5120, e5 | chunk1, srd);
    if (lane < 16) dma16(dst + 6144, e6 | chunk0, srd);
  };
  auto dmaB = [&]() {
    dma16(histl + kHistB, eB[0], srdB);
    dma16(histl + kHistB + 1024, eB[1], srdB);
    dma16(histl + kHistB + 2048, eB[2], srdB);
    dma16(histl + kHistB + 3072, eB[3], srdB);
  };
  STAMP(3);
  asm volatile("s_waitcnt vmcnt(4)" : "+v"(t32), "+v"(aux), "+v"(wa), "+v"(wc), "+v"(wd)::"memory");
  STAMP(4);
  if (nA > 32) rare(lo_, histl, srdA);
  if (nB > 32) rare(lo_ + 256, histl + kHistB, srdB);
  asm volatile("" ::: "memory");
  {
    const f32x2 k2 = {kNegLog2e, kNegLog2e};
    lds_st<f32x2>(tc + lane * 8, f32x2{lo2f(t32), hi2f(t32)} * k2);
    lds_st<unsigned>(lds + OFF_TB + (2 * w + (lane >> 5)) * 144 + (lane & 31) * 4, t32);
    const int hp = lane & 31;
    const int off = (hp >> 3) * 4096 + ((w >> 2) * 2 + (lane >> 5)) * 1024 + ((w & 3) * 16 + (hp & 7)) * 16;
    const f32x4 s0 = (f32x4{lo2f(wa[0]), lo2f(wa[1]), lo2f(wa[2]), lo2f(wa[3])} + f32x4{lo2f(wc[0]), lo2f(wc[1]), lo2f(wc[2]), lo2f(wc[3])}) * kNegLog2e;
    const f32x4 s1 = (f32x4{hi2f(wa[0]), hi2f(wa[1]), hi2f(wa[2]), hi2f(wa[3])} + f32x4{hi2f(wc[0]), hi2f(wc[1]), hi2f(wc[2]), hi2f(wc[3])}) * kNegLog2e;
    lds_st<f32x4>(lds + OFF_WAC + off, s0);
    lds_st<f32x4>(lds + OFF_WAC + off + 128, s1);
    lds_st<f32x4>(lds + OFF_WD + off, f32x4{lo2f(wd[0]), lo2f(wd[1]), lo2f(wd[2]), lo2f(wd[3])});
    lds_st<f32x4>(lds + OFF_WD + off + 128, f32x4{hi2f(wd[0]), hi2f(wd[1]), hi2f(wd[2]), hi2f(wd[3])});
  }
  if (w >= 4) {
    const float sa = w == 4 ? 1.f : w == 5 ? kB2Scale : kNegLog2e, sb = w == 6 ? -kShift : 0.f;
    const float v = __builtin_fmaf(__uint_as_float(aux), sa, sb);
    lds_st<float>(lds + OFF_AUX + (w & 3) * 256 + lane * 4, w == 7 ? 0.f : v);
  }
  __builtin_amdgcn_wave_barrier();
  asm volatile("" ::: "memory");
  f32x4 tA[2][2], tB[2][2];
#pragma unroll
  for (int s = 0; s < 2; ++s)
#pragma unroll
    for (int jh = 0; jh < 2; ++jh) {
      tA[s][jh] = lds_ld<f32x4>(tc + (32 * s + 8 * q + 4 * jh) * 4);
      tB[s][jh] = lds_ld<f32x4>(tc + 256 + (32 * s + 8 * q + 4 * jh) * 4);
    }
  asm volatile("s_waitcnt lgkmcnt(0)\n\ts_barrier"
               : "+v"(tA[0][0]), "+v"(tA[0][1]), "+v"(tA[1][0]), "+v"(tA[1][1]), "+v"(tB[0][0]), "+v"(tB[0][1]), "+v"(tB[1][0]), "+v"(tB[1][1])::"memory");
  STAMP(5);
  dmaB();
  STAMP(6);

  if (w < 4) {
    LDS_AS char* base = lds + OFF_WBC + (8 * q + (r >> 2)) * 128 + w * 32 + (lane & 3) * 8;
    f32x4 accb = {0, 0, 0, 0}, accc = {0, 0, 0, 0};
    const f32x4 bb = lds_ld<f32x4>(lds + OFF_AUX + 512 + w * 64 + q * 16);
#pragma unroll
    for (int s = 0; s < 2; ++s) {
      const u32x2 x0 = tr_read(base + s * 4096), x1 = tr_read(base + s * 4096 + 512);
      const u32x2 y0 = tr_read(base + 8192 + s * 4096), y1 = tr_read(base + 8192 + s * 4096 + 512);
      const u32x4 tf = lds_ld<u32x4>(lds + OFF_TB + r * 144 + s * 64 + q * 16);
      accb = mfma32(u32x4{x0[0], x0[1], x1[0], x1[1]}, tf, accb);
      accc = mfma32(u32x4{y0[0], y0[1], y1[0], y1[1]}, tf, accc);
    }
    const f32x4 cv = (accb - accc) * kNegLog2e + bb;
    lds_st<f32x4>(lds + OFF_TC + r * 256 + w * 64 + q * 16, cv);
    asm volatile("" ::: "memory");
    lds_st<unsigned char>(lds + OFF_CNT + w, (unsigned char)1);
    asm volatile("" ::: "memory");
  }
  STAMP(7);
  u32x4 wbA[4][2], wbB[4][2];
  {
    LDS_AS char* pa = lds + lane * 16;
    f32x4 a[3][2], d[3][2];
    auto ld = [&](int k) {
      a[k % 3][0] = lds_ld<f32x4>(pa + OFF_WAC + k * 2048);
      d[k % 3][0] = lds_ld<f32x4>(pa + OFF_WD + k * 2048);
      a[k % 3][1] = lds_ld<f32x4>(pa + OFF_WAC + k * 2048 + 1024);
      d[k % 3][1] = lds_ld<f32x4>(pa + OFF_WD + k * 2048 + 1024);
    };
    ld(0);
    ld(1);
#pragma unroll
    for (int k = 0; k < 8; ++k) {
      if (k + 2 < 8) ld(k + 2);
      __builtin_amdgcn_sched_barrier(0);
      {
        const f32x4 v0 = __builtin_elementwise_fma(d[k % 3][0], tA[k & 1][0], a[k % 3][0]);
        const f32x4 v1 = __builtin_elementwise_fma(d[k % 3][1], tA[k & 1][1], a[k % 3][1]);
        wbA[k >> 1][k & 1] = u32x4{pk_bf16(lo2(v0)), pk_bf16(hi2(v0)), pk_bf16(lo2(v1)), pk_bf16(hi2(v1))};
        const f32x4 u0 = __builtin_elementwise_fma(d[k % 3][0], tB[k & 1][0], a[k % 3][0]);
        const f32x4 u1 = __builtin_elementwise_fma(d[k % 3][1], tB[k & 1][1], a[k % 3][1]);
        wbB[k >> 1][k & 1] = u32x4{pk_bf16(lo2(u0)), pk_bf16(hi2(u0)), pk_bf16(lo2(u1)), pk_bf16(hi2(u1))};
      }
      asm volatile("" : "+v"(wbA[k >> 1][k & 1]), "+v"(wbB[k >> 1][k & 1]));
      __builtin_amdgcn_sched_barrier(0);
    }
  }
  const int hoff = (2 * (r & 7) + (r >> 3)) * 4;
  f32x4 cA[4], cB[4];
  for (;;) {
    const unsigned cnt = lds_ldv<unsigned>(lds + OFF_CNT);
#pragma unroll
    for (int ht = 0; ht < 4; ++ht)
#pragma unroll
      for (int i = 0; i < 4; ++i) cA[ht][i] = lds_ldv<float>(tc + ht * 64 + hoff);
    if (__builtin_amdgcn_readfirstlane(cnt) == 0x01010101u) break;
  }
#pragma unroll
  for (int ht = 0; ht < 4; ++ht)
#pragma unroll
    for (int i = 0; i < 4; ++i) cB[ht][i] = lds_ldv<float>(tc + 256 + ht * 64 + hoff);
  f32x2 w2[4];
#pragma unroll
  for (int ht = 0; ht < 4; ++ht) w2[ht] = f32x2{lds_ldv<float>(lds + OFF_AUX + ht * 64 + hoff), lds_ldv<float>(lds + OFF_AUX + ht * 64 + hoff)};
  const f32x2 init = lds_ld<f32x2>(lds + OFF_AUX + 256);

  LDS_AS char* hl = lds + OFF_HIST + w * (2 * kHistB);
  LDS_AS char* lm = lds + OFF_LM + w * 512 + q * 8;
  const int rrow = ((r & 2) << 2) | ((r >> 2) << 1) | (r & 1);
  int roffA = rrow * kRowB + ((q ^ (rrow & 2) ^ ((rrow >> 1) & 4)) * 16);
  const int trow = ((lane & 8)) | (q << 1) | ((lane >> 2) & 1);
  int toffA = trow * kRowB + ((((lane >> 1) & 1) ^ (trow & 2) ^ ((trow >> 1) & 4)) * 16) + (lane & 1) * 8;
  asm volatile("" : "+v"(roffA), "+v"(toffA));
  STAMP(8);
  const float resA = run_batch<6, 4, 9>(hl, lm, nA, wbA, cA, w2, init, roffA, toffA, lane ST_PASS);
  const float resB = run_batch<2, 0, 11>(hl + kHistB, lm + 256, nB, wbB, cB, w2, init, roffA, toffA, lane ST_PASS);
  const unsigned pk = pk_bf16(f32x2{resA, resB});
  unsigned short* outb = out + (size_t)b * 64;
  asm volatile("global_store_short %0, %1, %2 sc0 sc1\n\tglobal_store_short_d16_hi %0, %1, %2 offset:128 sc0 sc1" :: "v"(lane*2), "v"(pk), "s"(outb) : "memory");
  STAMP_FLUSH(blockIdx.x * kWaves + w);
}

extern "C" void kernel_launch(void* const* d_in, const int* in_sizes, int n_in, void* d_out, int out_size, void* d_ws, size_t ws_size,
                              hipStream_t stream) {
  const int B = in_sizes[0] / 64;
  din_kernel<<<B / kSlots, kWaves * 64, 0, stream>>>((const unsigned short*)d_in[0], (const unsigned short*)d_in[1], (const unsigned short*)d_in[2],
                                                     (const unsigned short*)d_in[3], (const unsigned short*)d_in[4], (const unsigned short*)d_in[5],
                                                     (const unsigned short*)d_in[6], (unsigned short*)d_out DBG_PASS);
}
```

```cpp
#include <hip/hip_runtime.h>
#include <hip/hip_bf16.h>

__global__ __launch_bounds__(512) void din_kernel(const unsigned short* target, const unsigned short* hist, const unsigned short* mask,
                                                  const unsigned short* w1, const unsigned short* b1, const unsigned short* w2,
                                                  const unsigned short* b2, unsigned short* out) {
  __shared__ __attribute__((aligned(256))) char lds_raw[162816];
  register unsigned tid asm("v0") = threadIdx.x;
  asm volatile(
    "s_load_dwordx16 s[8:23], %0, 0x0\n"
    "v_readfirstlane_b32 s28, v0\n"
    "v_lshlrev_b32 v85, 4, v0\n"
    "s_getpc_b64 s[54:55]\n"
    "L_pc%=:\n"
    "v_and_b32 v23, 0x1f80, v85\n"
    "v_min_u32 v23, L_end%=-L_pc%=-4, v23\n"
    "global_load_dword v224, v23, s[54:55]\n"
    "v_and_b32 v1, 63, v0\n"
    "s_lshr_b32 s28, s28, 6\n"
    "s_lshl_b32 s31, %1, 3\n"
    "s_add_u32 s31, s31, s28\n"
    "v_min_u32 v80, 49, v1\n"
    "s_mul_i32 s36, s31, 200\n"
    "v_lshl_add_u32 v80, v80, 1, s36\n"
    "v_mov_b64 v[64:65], 0\n"
    "s_lshl_b32 s47, s31, 8\n"
    "v_lshl_add_u32 v88, v1, 2, s47\n"
    "s_waitcnt lgkmcnt(0)\n"
    "global_load_short_d16_hi v64, v80, s[12:13] nt\n"
    "global_load_short_d16_hi v65, v80, s[12:13] offset:100 nt\n"
    "global_load_dword v66, v88, s[8:9] nt\n"
    "v_lshlrev_b32 v84, 2, v1\n"
    "v_and_b32 v81, 0x1e00, v85\n"
    "v_and_b32 v23, 0x7c, v84\n"
    "v_or_b32 v81, v81, v23\n"
    "v_add_u32 v82, 0x4000, v81\n"
    "v_add_u32 v83, 0x6000, v81\n"
    "v_lshlrev_b32 v94, 1, v1\n"
    "v_and_b32 v23, 32, v94\n"
    "v_lshlrev_b32 v20, 4, v1\n"
    "v_xor_b32 v85, v20, v23\n"
    "s_lshl_b32 s37, s28, 11\n"
    "s_add_u32 m0, s37, 32768\n"
    "s_add_u32 s37, s37, 0x2000\n"
    "v_and_b32 v27, 0x70, v85\n"
    "v_add_u32 v85, s37, v85\n"
    "v_xor_b32 v86, 64, v85\n"
    "v_mov_b32 v67, 0\n"
    "v_add_u32 v15, s47, v94\n"
    "s_cmp_eq_u32 s28, 1\n"
    "s_cselect_b32 s37, 0, -1\n"
    "v_and_b32 v94, s37, v94\n"
    "s_cselect_b64 s[38:39], s[20:21], s[16:17]\n"
    "s_cselect_b32 s51, 0x5b800000, 1.0\n"
    "s_cmp_eq_u32 s28, 0\n"
    "s_cselect_b64 s[38:39], s[18:19], s[38:39]\n"
    "global_load_short_d16_hi v67, v94, s[38:39]\n"
    "s_lshl_b32 s49, s28, 9\n"
    "v_add_u32 v88, s49, v84\n"
    "v_bfrev_b32 v23, 1\n"
    "global_load_lds_dwordx4 v85, s[14:15]\n"
    "v_mov_b32 v95, 0\n"
    "ds_write2st64_b32 v88, v23, v95 offset0:192 offset1:208\n"
    "ds_write2st64_b32 v88, v23, v95 offset0:193 offset1:209\n"
    "global_load_lds_dwordx4 v86, s[14:15] offset:1024\n"
    "s_mul_i32 s36, s31, 12800\n"
    "v_lshl_add_u32 v87, v1, 7, s36\n"
    "v_add_u32 v86, 6400, v87\n"
    "global_load_dword v76, v83, s[14:15] offset:0\n"
    "s_mov_b32 s24, s10\n"
    "s_and_b32 s25, s11, 0xffff\n"
    "s_brev_b32 s26, -2\n"
    "global_load_dword v77, v83, s[14:15] offset:128\n"
    "s_mov_b32 s27, 0x20000\n"
    "v_lshrrev_b32 v23, 3, v1\n"
    "s_add_u32 s36, s49, 49152\n"
    "global_load_dword v78, v83, s[14:15] offset:256\n"
    "v_lshl_add_u32 v31, v23, 2, s36\n"
    "v_xor_b32 v28, 64, v27\n"
    "s_mul_i32 s46, s28, 12800\n"
    "global_load_dword v79, v83, s[14:15] offset:384\n"
    "s_add_u32 s46, s46, 60416\n"
    "s_mov_b32 s42, 0xbfb8aa3b\n"
    "v_lshrrev_b32 v21, 4, v1\n"
    "global_load_dword v68, v81, s[14:15] offset:0\n"
    "v_lshl_add_u32 v92, v1, 3, s46\n"
    "v_lshl_add_u32 v91, v21, 5, s46\n"
    "s_and_b32 s50, s28, 3\n"
    "global_load_dword v69, v81, s[14:15] offset:128\n"
    "s_lshl_b32 s37, s50, 5\n"
    "v_xor_b32 v93, s37, v84\n"
    "s_lshl_b32 s36, s28, 8\n"
    "global_load_dword v70, v81, s[14:15] offset:256\n"
    "v_add_u32 v93, s36, v93\n"
    "s_min_u32 s36, s36, 0x300\n"
    "v_add_u32 v89, s36, v84\n"
    "global_load_dword v71, v81, s[14:15] offset:384\n"
    "v_lshlrev_b32 v90, 9, v1\n"
    "v_and_b32 v90, 0x3000, v90\n"
    "v_and_b32 v23, 0x270, v20\n"
    "global_load_dword v72, v82, s[14:15] offset:0\n"
    "v_and_b32 v24, 0x200, v20\n"
    "v_add3_u32 v90, v90, v23, v24\n"
    "s_and_b32 s36, s49, 0x800\n"
    "global_load_dword v73, v82, s[14:15] offset:128\n"
    "s_lshl_b32 s48, s50, 8\n"
    "s_add_u32 s36, s36, s48\n"
    "v_add_u32 v90, s36, v90\n"
    "global_load_dword v74, v82, s[14:15] offset:256\n"
    "v_and_b32 v22, 15, v1\n"
    "global_load_dword v75, v82, s[14:15] offset:384\n"
    "s_cmp_lt_u32 s28, 4\n"
    "s_cbranch_scc1 L_pb1%=\n"
    "s_barrier\n"
    "L_pb1%=:\n"
    "s_waitcnt vmcnt(15)\n"
    "v_cmp_neq_f32 s[54:55], 0, v64\n"
    "v_cmp_neq_f32 s[56:57], 0, v65\n"
    "s_and_b32 s55, s55, 0x3ffff\n"
    "v_mbcnt_lo_u32_b32 v96, s54, 0\n"
    "s_and_b32 s57, s57, 0x3ffff\n"
    "v_mbcnt_hi_u32_b32 v96, s55, v96\n"
    "v_mbcnt_lo_u32_b32 v97, s56, 0\n"
    "v_lshl_add_u32 v96, v96, 2, s49\n"
    "v_mbcnt_hi_u32_b32 v97, s57, v97\n"
    "s_mov_b64 exec, s[54:55]\n"
    "ds_write2st64_b32 v96, v87, v64 offset0:192 offset1:208\n"
    "s_mov_b64 exec, -1\n"
    "ds_read2_b32 v[100:101], v31 offset0:0 offset1:8\n"
    "ds_read2_b32 v[102:103], v31 offset0:16 offset1:24\n"
    "v_lshl_add_u32 v97, v97, 2, s49\n"
    "s_mov_b64 exec, s[56:57]\n"
    "ds_write2st64_b32 v97, v86, v65 offset0:193 offset1:209\n"
    "s_mov_b64 exec, -1\n"
    "ds_read2_b32 v[16:17], v31 offset0:64 offset1:72\n"
    "ds_read2_b32 v[18:19], v31 offset0:80 offset1:88\n"
    "ds_read2_b32 v[120:121], v31 offset0:32 offset1:96\n"
    "v_lshlrev_b32 v98, 16, v66\n"
    "v_and_b32 v99, 0xffff0000, v66\n"
    "s_bcnt1_i32_b64 s29, s[54:55]\n"
    "v_pk_mul_f32 v[98:99], v[98:99], s[42:43] op_sel_hi:[1,0]\n"
    "s_bcnt1_i32_b64 s30, s[56:57]\n"
    "ds_write_b64 v92, v[98:99] offset:0\n"
    "ds_write_b32 v93, v66 offset:57344\n"
    "s_cmp_ge_u32 s28, 4\n"
    "s_cbranch_scc1 L_pb2%=\n"
    "s_barrier\n"
    "L_pb2%=:\n"
    "s_add_u32 m0, s46, 1280\n"
    "s_waitcnt lgkmcnt(6)\n"
    "v_or_b32 v100, v100, v27\n"
    "buffer_load_dwordx4 v100, s[24:27], 0 offen nt lds\n"
    "ds_read_b128 v[32:35], v91 offset:0\n"
    "ds_read_b128 v[36:39], v91 offset:16\n"
    "s_add_u32 m0, s46, 2304\n"
    "v_or_b32 v101, v101, v28\n"
    "buffer_load_dwordx4 v101, s[24:27], 0 offen nt lds\n"
    "ds_read_b128 v[48:51], v91 offset:256\n"
    "ds_read_b128 v[52:55], v91 offset:272\n"
    "s_add_u32 m0, s46, 3328\n"
    "s_waitcnt lgkmcnt(9)\n"
    "v_or_b32 v102, v102, v27\n"
    "buffer_load_dwordx4 v102, s[24:27], 0 offen nt lds\n"
    "ds_read_b128 v[40:43], v91 offset:128\n"
    "ds_read_b128 v[44:47], v91 offset:144\n"
    "s_add_u32 m0, s46, 4352\n"
    "v_or_b32 v103, v103, v28\n"
    "buffer_load_dwordx4 v103, s[24:27], 0 offen nt lds\n"
    "ds_read_b128 v[56:59], v91 offset:384\n"
    "ds_read_b128 v[60:63], v91 offset:400\n"
    "s_waitcnt lgkmcnt(12)\n"
    "v_or_b32 v16, v16, v27\n"
    "v_or_b32 v17, v17, v28\n"
    "s_waitcnt lgkmcnt(11)\n"
    "v_or_b32 v18, v18, v27\n"
    "v_or_b32 v19, v19, v28\n"
    "s_waitcnt lgkmcnt(10)\n"
    "v_or_b32 v120, v120, v28\n"
    "v_or_b32 v121, v121, v28\n"
    "s_max_u32 s48, s29, s30\n"
    "s_waitcnt vmcnt(12)\n"
    "s_cmp_lt_u32 s28, 4\n"
    "v_mul_f32 v67, s51, v67\n"
    "ds_write_b32 v89, v67 offset:59392\n"
    "v_lshlrev_b32 v100, 16, v76\n"
    "v_lshlrev_b32 v101, 16, v77\n"
    "v_lshlrev_b32 v102, 16, v78\n"
    "v_lshlrev_b32 v103, 16, v79\n"
    "v_and_b32 v76, 0xffff0000, v76\n"
    "v_and_b32 v77, 0xffff0000, v77\n"
    "v_and_b32 v78, 0xffff0000, v78\n"
    "v_and_b32 v79, 0xffff0000, v79\n"
    "s_cbranch_scc1 L_nb0%=\n"
    "s_waitcnt lgkmcnt(0)\n"
    "s_barrier\n"
    "L_nb0%=:\n"
    "ds_write_b128 v90, v[100:103] offset:16384\n"
    "ds_write_b128 v90, v[76:79] offset:16512\n"
    "s_waitcnt vmcnt(8)\n"
    "v_lshlrev_b32 v104, 16, v68\n"
    "v_lshlrev_b32 v105, 16, v69\n"
    "v_lshlrev_b32 v106, 16, v70\n"
    "v_lshlrev_b32 v107, 16, v71\n"
    "v_and_b32 v68, 0xffff0000, v68\n"
    "v_and_b32 v69, 0xffff0000, v69\n"
    "v_and_b32 v70, 0xffff0000, v70\n"
    "v_and_b32 v71, 0xffff0000, v71\n"
    "s_waitcnt vmcnt(4)\n"
    "v_lshlrev_b32 v108, 16, v72\n"
    "v_lshlrev_b32 v109, 16, v73\n"
    "v_lshlrev_b32 v110, 16, v74\n"
    "v_lshlrev_b32 v111, 16, v75\n"
    "v_pk_add_f32 v[104:105], v[104:105], v[108:109]\n"
    "v_pk_add_f32 v[106:107], v[106:107], v[110:111]\n"
    "v_and_b32 v72, 0xffff0000, v72\n"
    "v_and_b32 v73, 0xffff0000, v73\n"
    "v_pk_mul_f32 v[104:105], v[104:105], s[42:43] op_sel_hi:[1,0]\n"
    "v_pk_mul_f32 v[106:107], v[106:107], s[42:43] op_sel_hi:[1,0]\n"
    "v_and_b32 v74, 0xffff0000, v74\n"
    "v_and_b32 v75, 0xffff0000, v75\n"
    "ds_write_b128 v90, v[104:107] offset:0\n"
    "v_pk_add_f32 v[68:69], v[68:69], v[72:73]\n"
    "v_pk_add_f32 v[70:71], v[70:71], v[74:75]\n"
    "v_pk_mul_f32 v[68:69], v[68:69], s[42:43] op_sel_hi:[1,0]\n"
    "v_pk_mul_f32 v[70:71], v[70:71], s[42:43] op_sel_hi:[1,0]\n"
    "ds_write_b128 v90, v[68:71] offset:128\n"
    "s_cbranch_scc0 L_noc%=\n"
    "v_lshrrev_b32 v23, 2, v22\n"
    "v_lshl_add_u32 v112, v21, 3, v23\n"
    "v_and_b32 v24, 0x60, v84\n"
    "v_xor_b32 v24, s37, v24\n"
    "v_lshl_add_u32 v112, v112, 7, v24\n"
    "v_and_b32 v23, 3, v1\n"
    "v_lshl_add_u32 v112, v23, 3, v112\n"
    "v_and_b32 v24, 6, v22\n"
    "v_xor_b32 v24, v24, v21\n"
    "v_lshlrev_b32 v24, 4, v24\n"
    "v_lshl_add_u32 v113, v22, 7, v24\n"
    "v_xor_b32 v96, 64, v113\n"
    "v_and_b32 v24, 48, v1\n"
    "s_lshl_b32 s36, s28, 6\n"
    "v_add_u32 v114, s36, v24\n"
    "s_waitcnt lgkmcnt(0)\n"
    "s_barrier\n"
    "ds_read_b64_tr_b16 v[128:129], v112 offset:32768\n"
    "ds_read_b64_tr_b16 v[130:131], v112 offset:33280\n"
    "ds_read_b64_tr_b16 v[132:133], v112 offset:36864\n"
    "ds_read_b64_tr_b16 v[134:135], v112 offset:37376\n"
    "ds_read_b128 v[144:147], v113 offset:57344\n"
    "ds_read_b128 v[152:155], v114 offset:59904\n"
    "ds_read_b128 v[148:151], v96 offset:57344\n"
    "ds_read_b64_tr_b16 v[136:137], v112 offset:40960\n"
    "ds_read_b64_tr_b16 v[138:139], v112 offset:41472\n"
    "ds_read_b64_tr_b16 v[140:141], v112 offset:45056\n"
    "ds_read_b64_tr_b16 v[142:143], v112 offset:45568\n"
    "v_lshrrev_b32 v23, 1, v22\n"
    "v_lshlrev_b32 v24, 4, v23\n"
    "v_xor_b32 v24, v24, v114\n"
    "v_mul_u32_u24 v23, 12288, v23\n"
    "v_lshl_add_u32 v115, v22, 8, v24\n"
    "v_add_u32 v115, v115, v23\n"
    "v_mov_b32 v98, 0xc2700000\n"
    "s_waitcnt lgkmcnt(5)\n"
    "v_mfma_f32_16x16x32_bf16 v[208:211], v[128:131], v[144:147], v[152:155]\n"
    "s_waitcnt lgkmcnt(4)\n"
    "v_mfma_f32_16x16x32_bf16 v[208:211], v[132:135], v[148:151], v[208:211]\n"
    "s_waitcnt lgkmcnt(2)\n"
    "v_mfma_f32_16x16x32_bf16 v[212:215], v[136:139], v[144:147], 0\n"
    "s_waitcnt lgkmcnt(0)\n"
    "v_mfma_f32_16x16x32_bf16 v[212:215], v[140:143], v[148:151], v[212:215]\n"
    "s_nop 7\n"
    "v_pk_add_f32 v[208:209], v[208:209], v[212:213] neg_lo:[0,1] neg_hi:[0,1]\n"
    "v_pk_add_f32 v[210:211], v[210:211], v[214:215] neg_lo:[0,1] neg_hi:[0,1]\n"
    "v_pk_fma_f32 v[208:209], v[208:209], s[42:43], v[98:99] op_sel_hi:[1,0,0]\n"
    "v_pk_fma_f32 v[210:211], v[210:211], s[42:43], v[98:99] op_sel_hi:[1,0,0]\n"
    "ds_write_b128 v115, v[208:211] offset:60928\n"
    "L_noc%=:\n"
    "s_waitcnt lgkmcnt(0)\n"
    "s_barrier\n"
    "ds_read_b128 v[160:163], v20 offset:0\n"
    "ds_read_b128 v[164:167], v20 offset:16384\n"
    "ds_read_b128 v[168:171], v20 offset:1024\n"
    "ds_read_b128 v[172:175], v20 offset:17408\n"
    "ds_read_b128 v[176:179], v20 offset:2048\n"
    "ds_read_b128 v[180:183], v20 offset:18432\n"
    "ds_read_b128 v[184:187], v20 offset:3072\n"
    "ds_read_b128 v[188:191], v20 offset:19456\n"
    "s_setprio 3\n"
    "s_add_u32 m0, s46, 5376\n"
    "s_cmp_gt_u32 s29, 32\n"
    "s_cselect_b64 exec, -1, 0\n"
    "buffer_load_dwordx4 v120, s[24:27], 0 offen nt lds\n"
    "s_add_u32 m0, s46, 11776\n"
    "s_cmp_gt_u32 s30, 32\n"
    "s_cselect_b64 exec, -1, 0\n"
    "buffer_load_dwordx4 v121, s[24:27], 0 offen nt lds\n"
    "s_mov_b64 exec, -1\n"
    "v_and_b32 v23, 2, v22\n"
    "v_lshlrev_b32 v8, 2, v23\n"
    "v_lshrrev_b32 v24, 1, v22\n"
    "v_and_or_b32 v8, v24, 6, v8\n"
    "v_and_or_b32 v8, v22, 1, v8\n"
    "v_lshl_add_u32 v8, v8, 7, s46\n"
    "v_and_b32 v24, 2, v24\n"
    "v_lshl_or_b32 v23, v23, 1, v24\n"
    "v_xor_b32 v23, v23, v21\n"
    "v_lshl_add_u32 v8, v23, 4, v8\n"
    "v_xor_b32 v9, 64, v8\n"
    "v_and_b32 v23, 7, v1\n"
    "v_lshrrev_b32 v24, 3, v22\n"
    "v_lshl_add_u32 v23, v23, 1, v24\n"
    "v_lshlrev_b32 v26, 2, v23\n"
    "s_lshl_b32 s36, s28, 4\n"
    "s_add_u32 s36, s36, s46\n"
    "s_add_u32 s36, s36, 512\n"
    "v_xor_b32 v25, s36, v26\n"
    "v_xor_b32 v3, 64, v25\n"
    "v_mov_b32 v29, 0\n"
    "s_mov_b32 s40, 0x21800000\n"
    "v_lshl_add_u32 v14, v21, 3, s49\n"
    "ds_read_b128 v[192:195], v20 offset:4096\n"
    "ds_read_b128 v[196:199], v20 offset:20480\n"
    "ds_read_b128 v[200:203], v20 offset:5120\n"
    "ds_read_b128 v[204:207], v20 offset:21504\n"
    "s_waitcnt lgkmcnt(10)\n"
    "v_pk_fma_f32 v[240:241], v[164:165], v[32:33], v[160:161]\n"
    "v_pk_fma_f32 v[242:243], v[166:167], v[34:35], v[162:163]\n"
    "s_waitcnt lgkmcnt(8)\n"
    "v_pk_fma_f32 v[244:245], v[172:173], v[36:37], v[168:169]\n"
    "v_pk_fma_f32 v[246:247], v[174:175], v[38:39], v[170:171]\n"
    "v_cvt_pk_bf16_f32 v64, v240, v241\n"
    "v_cvt_pk_bf16_f32 v65, v242, v243\n"
    "v_cvt_pk_bf16_f32 v66, v244, v245\n"
    "v_cvt_pk_bf16_f32 v67, v246, v247\n"
    "v_pk_fma_f32 v[240:241], v[164:165], v[48:49], v[160:161]\n"
    "v_pk_fma_f32 v[242:243], v[166:167], v[50:51], v[162:163]\n"
    "v_pk_fma_f32 v[244:245], v[172:173], v[52:53], v[168:169]\n"
    "v_pk_fma_f32 v[246:247], v[174:175], v[54:55], v[170:171]\n"
    "v_cvt_pk_bf16_f32 v96, v240, v241\n"
    "v_cvt_pk_bf16_f32 v97, v242, v243\n"
    "v_cvt_pk_bf16_f32 v98, v244, v245\n"
    "v_cvt_pk_bf16_f32 v99, v246, v247\n"
    "ds_read_b128 v[160:163], v20 offset:6144\n"
    "ds_read_b128 v[164:167], v20 offset:22528\n"
    "ds_read_b128 v[168:171], v20 offset:7168\n"
    "ds_read_b128 v[172:175], v20 offset:23552\n"
    "s_add_u32 m0, s46, 7680\n"
    "s_nop 0\n"
    "buffer_load_dwordx4 v16, s[24:27], 0 offen nt lds\n"
    "s_waitcnt lgkmcnt(10)\n"
    "v_pk_fma_f32 v[240:241], v[180:181], v[40:41], v[176:177]\n"
    "v_pk_fma_f32 v[242:243], v[182:183], v[42:43], v[178:179]\n"
    "s_waitcnt lgkmcnt(8)\n"
    "v_pk_fma_f32 v[244:245], v[188:189], v[44:45], v[184:185]\n"
    "v_pk_fma_f32 v[246:247], v[190:191], v[46:47], v[186:187]\n"
    "v_cvt_pk_bf16_f32 v68, v240, v241\n"
    "v_cvt_pk_bf16_f32 v69, v242, v243\n"
    "v_cvt_pk_bf16_f32 v70, v244, v245\n"
    "v_cvt_pk_bf16_f32 v71, v246, v247\n"
    "v_pk_fma_f32 v[240:241], v[180:181], v[56:57], v[176:177]\n"
    "v_pk_fma_f32 v[242:243], v[182:183], v[58:59], v[178:179]\n"
    "v_pk_fma_f32 v[244:245], v[188:189], v[60:61], v[184:185]\n"
    "v_pk_fma_f32 v[246:247], v[190:191], v[62:63], v[186:187]\n"
    "v_cvt_pk_bf16_f32 v100, v240, v241\n"
    "v_cvt_pk_bf16_f32 v101, v242, v243\n"
    "v_cvt_pk_bf16_f32 v102, v244, v245\n"
    "v_cvt_pk_bf16_f32 v103, v246, v247\n"
    "ds_read_b128 v[176:179], v20 offset:8192\n"
    "ds_read_b128 v[180:183], v20 offset:24576\n"
    "ds_read_b128 v[184:187], v20 offset:9216\n"
    "ds_read_b128 v[188:191], v20 offset:25600\n"
    "s_waitcnt lgkmcnt(10)\n"
    "v_pk_fma_f32 v[240:241], v[196:197], v[32:33], v[192:193]\n"
    "v_pk_fma_f32 v[242:243], v[198:199], v[34:35], v[194:195]\n"
    "s_waitcnt lgkmcnt(8)\n"
    "v_pk_fma_f32 v[244:245], v[204:205], v[36:37], v[200:201]\n"
    "v_pk_fma_f32 v[246:247], v[206:207], v[38:39], v[202:203]\n"
    "v_cvt_pk_bf16_f32 v72, v240, v241\n"
    "v_cvt_pk_bf16_f32 v73, v242, v243\n"
    "v_cvt_pk_bf16_f32 v74, v244, v245\n"
    "v_cvt_pk_bf16_f32 v75, v246, v247\n"
    "v_pk_fma_f32 v[240:241], v[196:197], v[48:49], v[192:193]\n"
    "v_pk_fma_f32 v[242:243], v[198:199], v[50:51], v[194:195]\n"
    "v_pk_fma_f32 v[244:245], v[204:205], v[52:53], v[200:201]\n"
    "v_pk_fma_f32 v[246:247], v[206:207], v[54:55], v[202:203]\n"
    "v_cvt_pk_bf16_f32 v104, v240, v241\n"
    "v_cvt_pk_bf16_f32 v105, v242, v243\n"
    "v_cvt_pk_bf16_f32 v106, v244, v245\n"
    "v_cvt_pk_bf16_f32 v107, v246, v247\n"
    "ds_read_b128 v[192:195], v20 offset:10240\n"
    "ds_read_b128 v[196:199], v20 offset:26624\n"
    "ds_read_b128 v[200:203], v20 offset:11264\n"
    "ds_read_b128 v[204:207], v20 offset:27648\n"
    "s_add_u32 m0, s46, 8704\n"
    "s_nop 0\n"
    "buffer_load_dwordx4 v17, s[24:27], 0 offen nt lds\n"
    "s_waitcnt lgkmcnt(10)\n"
    "v_pk_fma_f32 v[240:241], v[164:165], v[40:41], v[160:161]\n"
    "v_pk_fma_f32 v[242:243], v[166:167], v[42:43], v[162:163]\n"
    "s_waitcnt lgkmcnt(8)\n"
    "v_pk_fma_f32 v[244:245], v[172:173], v[44:45], v[168:169]\n"
    "v_pk_fma_f32 v[246:247], v[174:175], v[46:47], v[170:171]\n"
    "v_cvt_pk_bf16_f32 v76, v240, v241\n"
    "v_cvt_pk_bf16_f32 v77, v242, v243\n"
    "v_cvt_pk_bf16_f32 v78, v244, v245\n"
    "v_cvt_pk_bf16_f32 v79, v246, v247\n"
    "v_pk_fma_f32 v[240:241], v[164:165], v[56:57], v[160:161]\n"
    "v_pk_fma_f32 v[242:243], v[166:167], v[58:59], v[162:163]\n"
    "v_pk_fma_f32 v[244:245], v[172:173], v[60:61], v[168:169]\n"
    "v_pk_fma_f32 v[246:247], v[174:175], v[62:63], v[170:171]\n"
    "v_cvt_pk_bf16_f32 v108, v240, v241\n"
    "v_cvt_pk_bf16_f32 v109, v242, v243\n"
    "v_cvt_pk_bf16_f32 v110, v244, v245\n"
    "v_cvt_pk_bf16_f32 v111, v246, v247\n"
    "ds_read_b128 v[160:163], v20 offset:12288\n"
    "ds_read_b128 v[164:167], v20 offset:28672\n"
    "ds_read_b128 v[168:171], v20 offset:13312\n"
    "ds_read_b128 v[172:175], v20 offset:29696\n"
    "s_waitcnt lgkmcnt(10)\n"
    "v_pk_fma_f32 v[240:241], v[180:181], v[32:33], v[176:177]\n"
    "v_pk_fma_f32 v[242:243], v[182:183], v[34:35], v[178:179]\n"
    "s_waitcnt lgkmcnt(8)\n"
    "v_pk_fma_f32 v[244:245], v[188:189], v[36:37], v[184:185]\n"
    "v_pk_fma_f32 v[246:247], v[190:191], v[38:39], v[186:187]\n"
    "v_cvt_pk_bf16_f32 v80, v240, v241\n"
    "v_cvt_pk_bf16_f32 v81, v242, v243\n"
    "v_cvt_pk_bf16_f32 v82, v244, v245\n"
    "v_cvt_pk_bf16_f32 v83, v246, v247\n"
    "v_pk_fma_f32 v[240:241], v[180:181], v[48:49], v[176:177]\n"
    "v_pk_fma_f32 v[242:243], v[182:183], v[50:51], v[178:179]\n"
    "v_pk_fma_f32 v[244:245], v[188:189], v[52:53], v[184:185]\n"
    "v_pk_fma_f32 v[246:247], v[190:191], v[54:55], v[186:187]\n"
    "v_cvt_pk_bf16_f32 v112, v240, v241\n"
    "v_cvt_pk_bf16_f32 v113, v242, v243\n"
    "v_cvt_pk_bf16_f32 v114, v244, v245\n"
    "v_cvt_pk_bf16_f32 v115, v246, v247\n"
    "ds_read_b128 v[176:179], v20 offset:14336\n"
    "ds_read_b128 v[180:183], v20 offset:30720\n"
    "ds_read_b128 v[184:187], v20 offset:15360\n"
    "ds_read_b128 v[188:191], v20 offset:31744\n"
    "s_add_u32 m0, s46, 9728\n"
    "s_nop 0\n"
    "buffer_load_dwordx4 v18, s[24:27], 0 offen nt lds\n"
    "s_waitcnt lgkmcnt(10)\n"
    "v_pk_fma_f32 v[240:241], v[196:197], v[40:41], v[192:193]\n"
    "v_pk_fma_f32 v[242:243], v[198:199], v[42:43], v[194:195]\n"
    "s_waitcnt lgkmcnt(8)\n"
    "v_pk_fma_f32 v[244:245], v[204:205], v[44:45], v[200:201]\n"
    "v_pk_fma_f32 v[246:247], v[206:207], v[46:47], v[202:203]\n"
    "v_cvt_pk_bf16_f32 v84, v240, v241\n"
    "v_cvt_pk_bf16_f32 v85, v242, v243\n"
    "v_cvt_pk_bf16_f32 v86, v244, v245\n"
    "v_cvt_pk_bf16_f32 v87, v246, v247\n"
    "v_pk_fma_f32 v[240:241], v[196:197], v[56:57], v[192:193]\n"
    "v_pk_fma_f32 v[242:243], v[198:199], v[58:59], v[194:195]\n"
    "v_pk_fma_f32 v[244:245], v[204:205], v[60:61], v[200:201]\n"
    "v_pk_fma_f32 v[246:247], v[206:207], v[62:63], v[202:203]\n"
    "v_cvt_pk_bf16_f32 v116, v240, v241\n"
    "v_cvt_pk_bf16_f32 v117, v242, v243\n"
    "v_cvt_pk_bf16_f32 v118, v244, v245\n"
    "v_cvt_pk_bf16_f32 v119, v246, v247\n"
    "s_add_u32 m0, s46, 10752\n"
    "s_nop 0\n"
    "buffer_load_dwordx4 v19, s[24:27], 0 offen nt lds\n"
    "ds_read2_b32 v[128:129], v25 offset0:0 offset1:0\n"
    "ds_read2_b32 v[132:133], v3 offset0:0 offset1:0\n"
    "ds_read2_b32 v[136:137], v25 offset0:32 offset1:32\n"
    "ds_read2_b32 v[140:141], v3 offset0:32 offset1:32\n"
    "ds_read2_b32 v[144:145], v25 offset0:64 offset1:64\n"
    "ds_read2_b32 v[148:149], v3 offset0:64 offset1:64\n"
    "ds_read2_b32 v[152:153], v25 offset0:96 offset1:96\n"
    "s_waitcnt lgkmcnt(14)\n"
    "ds_read2_b32 v[156:157], v3 offset0:96 offset1:96\n"
    "s_waitcnt lgkmcnt(14)\n"
    "v_pk_fma_f32 v[240:241], v[164:165], v[32:33], v[160:161]\n"
    "v_pk_fma_f32 v[242:243], v[166:167], v[34:35], v[162:163]\n"
    "s_waitcnt lgkmcnt(12)\n"
    "v_pk_fma_f32 v[244:245], v[172:173], v[36:37], v[168:169]\n"
    "v_pk_fma_f32 v[246:247], v[174:175], v[38:39], v[170:171]\n"
    "v_cvt_pk_bf16_f32 v88, v240, v241\n"
    "v_cvt_pk_bf16_f32 v89, v242, v243\n"
    "v_cvt_pk_bf16_f32 v90, v244, v245\n"
    "v_cvt_pk_bf16_f32 v91, v246, v247\n"
    "v_pk_fma_f32 v[240:241], v[164:165], v[48:49], v[160:161]\n"
    "v_pk_fma_f32 v[242:243], v[166:167], v[50:51], v[162:163]\n"
    "v_pk_fma_f32 v[244:245], v[172:173], v[52:53], v[168:169]\n"
    "v_pk_fma_f32 v[246:247], v[174:175], v[54:55], v[170:171]\n"
    "v_cvt_pk_bf16_f32 v120, v240, v241\n"
    "v_cvt_pk_bf16_f32 v121, v242, v243\n"
    "v_cvt_pk_bf16_f32 v122, v244, v245\n"
    "v_cvt_pk_bf16_f32 v123, v246, v247\n"
    "s_waitcnt vmcnt(6)\n"
    "ds_read_b128 v[192:195], v8 offset:1280\n"
    "ds_read_b128 v[196:199], v9 offset:1280\n"
    "ds_read_b32 v252, v26 offset:59392\n"
    "s_waitcnt lgkmcnt(14)\n"
    "ds_read_b32 v28, v26 offset:59456\n"
    "s_waitcnt lgkmcnt(14)\n"
    "ds_read_b32 v254, v26 offset:59520\n"
    "s_waitcnt lgkmcnt(14)\n"
    "ds_read_b32 v0, v26 offset:59584\n"
    "s_waitcnt lgkmcnt(14)\n"
    "ds_read_b32 v2, v26 offset:59648\n"
    "v_pk_fma_f32 v[240:241], v[180:181], v[40:41], v[176:177]\n"
    "v_pk_fma_f32 v[242:243], v[182:183], v[42:43], v[178:179]\n"
    "v_pk_fma_f32 v[244:245], v[188:189], v[44:45], v[184:185]\n"
    "v_pk_fma_f32 v[246:247], v[190:191], v[46:47], v[186:187]\n"
    "v_cvt_pk_bf16_f32 v92, v240, v241\n"
    "v_cvt_pk_bf16_f32 v93, v242, v243\n"
    "v_cvt_pk_bf16_f32 v94, v244, v245\n"
    "v_cvt_pk_bf16_f32 v95, v246, v247\n"
    "v_pk_fma_f32 v[240:241], v[180:181], v[56:57], v[176:177]\n"
    "v_pk_fma_f32 v[242:243], v[182:183], v[58:59], v[178:179]\n"
    "v_pk_fma_f32 v[244:245], v[188:189], v[60:61], v[184:185]\n"
    "v_pk_fma_f32 v[246:247], v[190:191], v[62:63], v[186:187]\n"
    "v_cvt_pk_bf16_f32 v124, v240, v241\n"
    "v_cvt_pk_bf16_f32 v125, v242, v243\n"
    "v_cvt_pk_bf16_f32 v126, v244, v245\n"
    "v_cvt_pk_bf16_f32 v127, v246, v247\n"
    "s_waitcnt lgkmcnt(14)\n"
    "ds_read_b64 v[240:241], v14 offset:53248\n"
    "s_waitcnt lgkmcnt(14)\n"
    "ds_read_b64 v[242:243], v14 offset:53280\n"
    "s_waitcnt lgkmcnt(14)\n"
    "ds_read_b64 v[244:245], v14 offset:53312\n"
    "s_waitcnt lgkmcnt(14)\n"
    "ds_read_b64 v[246:247], v14 offset:53344\n"
    "v_mov_b64 v[130:131], v[128:129]\n"
    "v_mov_b64 v[134:135], v[132:133]\n"
    "v_mov_b64 v[138:139], v[136:137]\n"
    "v_mov_b64 v[142:143], v[140:141]\n"
    "s_waitcnt lgkmcnt(14)\n"
    "v_mov_b64 v[146:147], v[144:145]\n"
    "s_waitcnt lgkmcnt(13)\n"
    "v_mov_b64 v[150:151], v[148:149]\n"
    "s_waitcnt lgkmcnt(12)\n"
    "v_mov_b64 v[154:155], v[152:153]\n"
    "s_waitcnt lgkmcnt(11)\n"
    "v_mov_b64 v[158:159], v[156:157]\n"
    "s_cmp_gt_u32 s48, 32\n"
    "s_cbranch_scc1 L_rw%=\n"
    "s_waitcnt vmcnt(4)\n"
    "L_rw%=:\n"
    "s_waitcnt vmcnt(5)\n"
    "ds_read_b128 v[200:203], v8 offset:3328\n"
    "ds_read_b128 v[204:207], v9 offset:3328\n"
    "s_setprio 2\n"
    "s_waitcnt lgkmcnt(12)\n"
    "v_mfma_f32_16x16x32_bf16 v[160:163], v[192:195], v[64:67], v[128:131]\n"
    "v_and_b32 v23, 8, v1\n"
    "v_lshl_or_b32 v10, v21, 1, v23\n"
    "v_mfma_f32_16x16x32_bf16 v[164:167], v[192:195], v[72:75], v[132:135]\n"
    "v_bfe_u32 v24, v1, 2, 1\n"
    "v_or_b32 v10, v10, v24\n"
    "v_mfma_f32_16x16x32_bf16 v[168:171], v[192:195], v[80:83], v[136:139]\n"
    "v_lshrrev_b32 v23, 1, v23\n"
    "v_and_or_b32 v24, v10, 2, v23\n"
    "v_mfma_f32_16x16x32_bf16 v[172:175], v[192:195], v[88:91], v[140:143]\n"
    "v_lshl_add_u32 v10, v10, 7, s46\n"
    "v_lshl_add_u32 v10, v24, 4, v10\n"
    "s_waitcnt lgkmcnt(11)\n"
    "v_mfma_f32_16x16x32_bf16 v[160:163], v[196:199], v[68:71], v[160:163]\n"
    "v_and_b32 v23, 3, v1\n"
    "v_lshl_add_u32 v10, v23, 3, v10\n"
    "v_mfma_f32_16x16x32_bf16 v[164:167], v[196:199], v[76:79], v[164:167]\n"
    "v_xor_b32 v11, 32, v10\n"
    "v_xor_b32 v12, 64, v10\n"
    "v_mfma_f32_16x16x32_bf16 v[168:171], v[196:199], v[84:87], v[168:171]\n"
    "v_xor_b32 v13, 96, v10\n"
    "v_mov_b32 v251, 0\n"
    "v_mfma_f32_16x16x32_bf16 v[172:175], v[196:199], v[92:95], v[172:175]\n"
    "v_mov_b32 v27, 0\n"
    "v_mov_b32 v30, 0\n"
    "ds_read_b64_tr_b16 v[208:209], v10 offset:1280\n"
    "ds_read_b64_tr_b16 v[210:211], v10 offset:3328\n"
    "ds_read_b64_tr_b16 v[212:213], v11 offset:1280\n"
    "ds_read_b64_tr_b16 v[214:215], v11 offset:3328\n"
    "s_waitcnt lgkmcnt(14)\n"
    "ds_read_b64_tr_b16 v[216:217], v12 offset:1280\n"
    "s_waitcnt lgkmcnt(14)\n"
    "ds_read_b64_tr_b16 v[218:219], v12 offset:3328\n"
    "s_waitcnt lgkmcnt(14)\n"
    "ds_read_b64_tr_b16 v[220:221], v13 offset:1280\n"
    "s_waitcnt lgkmcnt(14)\n"
    "ds_read_b64_tr_b16 v[222:223], v13 offset:3328\n"
    "s_waitcnt lgkmcnt(14)\n"
    "ds_read_b64 v[16:17], v14 offset:53504\n"
    "s_waitcnt lgkmcnt(14)\n"
    "ds_read_b64 v[18:19], v14 offset:53536\n"
    "s_waitcnt lgkmcnt(14)\n"
    "ds_read_b64 v[20:21], v14 offset:53568\n"
    "s_waitcnt lgkmcnt(14)\n"
    "ds_read_b64 v[22:23], v14 offset:53600\n"
    "s_waitcnt lgkmcnt(13)\n"
    "v_mfma_f32_16x16x32_bf16 v[176:179], v[200:203], v[64:67], v[128:131]\n"
    "v_exp_f32_e64 v160, v160 clamp\n"
    "v_mfma_f32_16x16x32_bf16 v[180:183], v[200:203], v[72:75], v[132:135]\n"
    "v_exp_f32_e64 v161, v161 clamp\n"
    "v_mfma_f32_16x16x32_bf16 v[184:187], v[200:203], v[80:83], v[136:139]\n"
    "v_exp_f32_e64 v164, v164 clamp\n"
    "v_mfma_f32_16x16x32_bf16 v[188:191], v[200:203], v[88:91], v[140:143]\n"
    "v_exp_f32_e64 v165, v165 clamp\n"
    "s_waitcnt lgkmcnt(12)\n"
    "v_mfma_f32_16x16x32_bf16 v[176:179], v[204:207], v[68:71], v[176:179]\n"
    "v_exp_f32_e64 v168, v168 clamp\n"
    "v_mfma_f32_16x16x32_bf16 v[180:183], v[204:207], v[76:79], v[180:183]\n"
    "v_exp_f32_e64 v169, v169 clamp\n"
    "v_mfma_f32_16x16x32_bf16 v[184:187], v[204:207], v[84:87], v[184:187]\n"
    "v_exp_f32_e64 v172, v172 clamp\n"
    "v_mfma_f32_16x16x32_bf16 v[188:191], v[204:207], v[92:95], v[188:191]\n"
    "v_exp_f32_e64 v173, v173 clamp\n"
    "v_exp_f32_e64 v162, v162 clamp\n"
    "v_exp_f32_e64 v163, v163 clamp\n"
    "v_exp_f32_e64 v166, v166 clamp\n"
    "v_exp_f32_e64 v167, v167 clamp\n"
    "v_exp_f32_e64 v170, v170 clamp\n"
    "v_exp_f32_e64 v171, v171 clamp\n"
    "v_exp_f32_e64 v174, v174 clamp\n"
    "v_exp_f32_e64 v175, v175 clamp\n"
    "v_pk_add_f32 v[160:161], v[160:161], s[40:41] op_sel_hi:[1,0]\n"
    "v_pk_add_f32 v[164:165], v[164:165], s[40:41] op_sel_hi:[1,0]\n"
    "v_pk_add_f32 v[168:169], v[168:169], s[40:41] op_sel_hi:[1,0]\n"
    "v_pk_add_f32 v[172:173], v[172:173], s[40:41] op_sel_hi:[1,0]\n"
    "v_pk_mul_f32 v[4:5], v[160:161], v[164:165]\n"
    "v_pk_mul_f32 v[6:7], v[168:169], v[172:173]\n"
    "v_pk_mul_f32 v[160:161], v[160:161], v[28:29] op_sel_hi:[1,0]\n"
    "v_rcp_f32 v4, v4\n"
    "v_rcp_f32 v5, v5\n"
    "v_pk_mul_f32 v[168:169], v[168:169], v[0:1] op_sel_hi:[1,0]\n"
    "v_pk_fma_f32 v[160:161], v[164:165], v[252:253], v[160:161] op_sel_hi:[1,0,1]\n"
    "v_rcp_f32 v6, v6\n"
    "v_rcp_f32 v7, v7\n"
    "v_pk_fma_f32 v[160:161], v[160:161], v[4:5], v[2:3] op_sel_hi:[1,1,0]\n"
    "v_pk_fma_f32 v[168:169], v[172:173], v[254:255], v[168:169] op_sel_hi:[1,0,1]\n"
    "v_pk_fma_f32 v[160:161], v[168:169], v[6:7], v[160:161]\n"
    "v_pk_mul_f32 v[160:161], v[160:161], v[240:241]\n"
    "v_cvt_pk_bf16_f32 v248, v160, v161\n"
    "v_pk_add_f32 v[162:163], v[162:163], s[40:41] op_sel_hi:[1,0]\n"
    "v_pk_add_f32 v[166:167], v[166:167], s[40:41] op_sel_hi:[1,0]\n"
    "v_pk_add_f32 v[170:171], v[170:171], s[40:41] op_sel_hi:[1,0]\n"
    "v_pk_add_f32 v[174:175], v[174:175], s[40:41] op_sel_hi:[1,0]\n"
    "v_pk_mul_f32 v[4:5], v[162:163], v[166:167]\n"
    "v_pk_mul_f32 v[6:7], v[170:171], v[174:175]\n"
    "v_pk_mul_f32 v[162:163], v[162:163], v[28:29] op_sel_hi:[1,0]\n"
    "v_rcp_f32 v4, v4\n"
    "v_rcp_f32 v5, v5\n"
    "v_pk_mul_f32 v[170:171], v[170:171], v[0:1] op_sel_hi:[1,0]\n"
    "v_pk_fma_f32 v[162:163], v[166:167], v[252:253], v[162:163] op_sel_hi:[1,0,1]\n"
    "v_rcp_f32 v6, v6\n"
    "v_rcp_f32 v7, v7\n"
    "v_pk_fma_f32 v[162:163], v[162:163], v[4:5], v[2:3] op_sel_hi:[1,1,0]\n"
    "v_pk_fma_f32 v[170:171], v[174:175], v[254:255], v[170:171] op_sel_hi:[1,0,1]\n"
    "v_pk_fma_f32 v[162:163], v[170:171], v[6:7], v[162:163]\n"
    "v_pk_mul_f32 v[162:163], v[162:163], v[242:243]\n"
    "v_cvt_pk_bf16_f32 v249, v162, v163\n"
    "s_cmp_le_u32 s29, 24\n"
    "s_waitcnt vmcnt(2)\n"
    "ds_read_b128 v[192:195], v8 offset:7680\n"
    "ds_read_b128 v[196:199], v9 offset:7680\n"
    "s_cbranch_scc1 L_skipA%=\n"
    "s_cmp_le_u32 s29, 32\n"
    "v_exp_f32_e64 v178, v178 clamp\n"
    "v_exp_f32_e64 v179, v179 clamp\n"
    "v_exp_f32_e64 v182, v182 clamp\n"
    "v_exp_f32_e64 v183, v183 clamp\n"
    "v_exp_f32_e64 v186, v186 clamp\n"
    "v_exp_f32_e64 v187, v187 clamp\n"
    "v_exp_f32_e64 v190, v190 clamp\n"
    "v_exp_f32_e64 v191, v191 clamp\n"
    "v_pk_add_f32 v[178:179], v[178:179], s[40:41] op_sel_hi:[1,0]\n"
    "v_pk_add_f32 v[182:183], v[182:183], s[40:41] op_sel_hi:[1,0]\n"
    "v_pk_add_f32 v[186:187], v[186:187], s[40:41] op_sel_hi:[1,0]\n"
    "v_pk_add_f32 v[190:191], v[190:191], s[40:41] op_sel_hi:[1,0]\n"
    "v_pk_mul_f32 v[4:5], v[178:179], v[182:183]\n"
    "v_pk_mul_f32 v[6:7], v[186:187], v[190:191]\n"
    "v_pk_mul_f32 v[178:179], v[178:179], v[28:29] op_sel_hi:[1,0]\n"
    "v_rcp_f32 v4, v4\n"
    "v_rcp_f32 v5, v5\n"
    "v_pk_mul_f32 v[186:187], v[186:187], v[0:1] op_sel_hi:[1,0]\n"
    "v_pk_fma_f32 v[178:179], v[182:183], v[252:253], v[178:179] op_sel_hi:[1,0,1]\n"
    "v_rcp_f32 v6, v6\n"
    "v_rcp_f32 v7, v7\n"
    "v_pk_fma_f32 v[178:179], v[178:179], v[4:5], v[2:3] op_sel_hi:[1,1,0]\n"
    "v_pk_fma_f32 v[186:187], v[190:191], v[254:255], v[186:187] op_sel_hi:[1,0,1]\n"
    "v_pk_fma_f32 v[178:179], v[186:187], v[6:7], v[178:179]\n"
    "v_pk_mul_f32 v[178:179], v[178:179], v[246:247]\n"
    "v_cvt_pk_bf16_f32 v251, v178, v179\n"
    "s_cbranch_scc1 L_skipA%=\n"
    "ds_read_b128 v[200:203], v8 offset:4352\n"
    "s_waitcnt lgkmcnt(14)\n"
    "ds_read_b128 v[204:207], v9 offset:4352\n"
    "s_waitcnt lgkmcnt(14)\n"
    "ds_read_b64 v[58:59], v14 offset:53376\n"
    "s_waitcnt lgkmcnt(14)\n"
    "ds_read_b64_tr_b16 v[48:49], v10 offset:4352\n"
    "s_waitcnt lgkmcnt(14)\n"
    "ds_read_b64_tr_b16 v[50:51], v11 offset:4352\n"
    "s_waitcnt lgkmcnt(14)\n"
    "ds_read_b64_tr_b16 v[52:53], v12 offset:4352\n"
    "s_waitcnt lgkmcnt(14)\n"
    "ds_read_b64_tr_b16 v[54:55], v13 offset:4352\n"
    "v_mov_b32 v60, 0\n"
    "s_waitcnt lgkmcnt(6)\n"
    "v_mfma_f32_16x16x32_bf16 v[160:163], v[200:203], v[64:67], v[128:131]\n"
    "v_mfma_f32_16x16x32_bf16 v[164:167], v[200:203], v[72:75], v[132:135]\n"
    "v_mfma_f32_16x16x32_bf16 v[168:171], v[200:203], v[80:83], v[136:139]\n"
    "v_mfma_f32_16x16x32_bf16 v[172:175], v[200:203], v[88:91], v[140:143]\n"
    "s_waitcnt lgkmcnt(5)\n"
    "v_mfma_f32_16x16x32_bf16 v[160:163], v[204:207], v[68:71], v[160:163]\n"
    "v_mfma_f32_16x16x32_bf16 v[164:167], v[204:207], v[76:79], v[164:167]\n"
    "v_mfma_f32_16x16x32_bf16 v[168:171], v[204:207], v[84:87], v[168:171]\n"
    "v_mfma_f32_16x16x32_bf16 v[172:175], v[204:207], v[92:95], v[172:175]\n"
    "s_nop 4\n"
    "v_exp_f32_e64 v162, v162 clamp\n"
    "v_exp_f32_e64 v163, v163 clamp\n"
    "v_exp_f32_e64 v166, v166 clamp\n"
    "v_exp_f32_e64 v167, v167 clamp\n"
    "v_exp_f32_e64 v170, v170 clamp\n"
    "v_exp_f32_e64 v171, v171 clamp\n"
    "v_exp_f32_e64 v174, v174 clamp\n"
    "v_exp_f32_e64 v175, v175 clamp\n"
    "v_pk_add_f32 v[162:163], v[162:163], s[40:41] op_sel_hi:[1,0]\n"
    "v_pk_add_f32 v[166:167], v[166:167], s[40:41] op_sel_hi:[1,0]\n"
    "v_pk_add_f32 v[170:171], v[170:171], s[40:41] op_sel_hi:[1,0]\n"
    "v_pk_add_f32 v[174:175], v[174:175], s[40:41] op_sel_hi:[1,0]\n"
    "v_pk_mul_f32 v[4:5], v[162:163], v[166:167]\n"
    "v_pk_mul_f32 v[6:7], v[170:171], v[174:175]\n"
    "v_pk_mul_f32 v[162:163], v[162:163], v[28:29] op_sel_hi:[1,0]\n"
    "v_rcp_f32 v4, v4\n"
    "v_rcp_f32 v5, v5\n"
    "v_pk_mul_f32 v[170:171], v[170:171], v[0:1] op_sel_hi:[1,0]\n"
    "v_pk_fma_f32 v[162:163], v[166:167], v[252:253], v[162:163] op_sel_hi:[1,0,1]\n"
    "v_rcp_f32 v6, v6\n"
    "v_rcp_f32 v7, v7\n"
    "v_pk_fma_f32 v[162:163], v[162:163], v[4:5], v[2:3] op_sel_hi:[1,1,0]\n"
    "v_pk_fma_f32 v[170:171], v[174:175], v[254:255], v[170:171] op_sel_hi:[1,0,1]\n"
    "v_pk_fma_f32 v[162:163], v[170:171], v[6:7], v[162:163]\n"
    "s_waitcnt lgkmcnt(4)\n"
    "v_pk_mul_f32 v[162:163], v[162:163], v[58:59]\n"
    "v_cvt_pk_bf16_f32 v61, v162, v163\n"
    "s_waitcnt lgkmcnt(3)\n"
    "s_nop 0\n"
    "v_mfma_f32_16x16x16_bf16 v[224:227], v[60:61], v[48:49], 0\n"
    "s_waitcnt lgkmcnt(2)\n"
    "v_mfma_f32_16x16x16_bf16 v[228:231], v[60:61], v[50:51], 0\n"
    "s_waitcnt lgkmcnt(1)\n"
    "v_mfma_f32_16x16x16_bf16 v[232:235], v[60:61], v[52:53], 0\n"
    "s_waitcnt lgkmcnt(0)\n"
    "v_mfma_f32_16x16x16_bf16 v[236:239], v[60:61], v[54:55], 0\n"
    "s_nop 1\n"
    "v_pk_add_f32 v[224:225], v[224:225], v[226:227]\n"
    "s_nop 0\n"
    "v_pk_add_f32 v[228:229], v[228:229], v[230:231]\n"
    "s_nop 0\n"
    "v_pk_add_f32 v[232:233], v[232:233], v[234:235]\n"
    "s_nop 0\n"
    "v_pk_add_f32 v[236:237], v[236:237], v[238:239]\n"
    "v_add_f32 v226, v224, v225\n"
    "v_add_f32 v227, v228, v229\n"
    "v_add_f32 v230, v232, v233\n"
    "v_add_f32 v231, v236, v237\n"
    "s_nop 0\n"
    "v_permlane32_swap_b32 v226, v230\n"
    "v_permlane32_swap_b32 v227, v231\n"
    "v_pk_add_f32 v[226:227], v[226:227], v[230:231]\n"
    "s_nop 1\n"
    "v_permlane16_swap_b32 v226, v227\n"
    "v_add_f32 v226, v226, v227\n"
    "v_add_f32 v29, v29, v226\n"
    "L_skipA%=:\n"
    "s_waitcnt lgkmcnt(1)\n"
    "v_mfma_f32_16x16x32_bf16 v[32:35], v[192:195], v[96:99], v[144:147]\n"
    "v_exp_f32_e64 v176, v176 clamp\n"
    "v_mfma_f32_16x16x32_bf16 v[36:39], v[192:195], v[104:107], v[148:151]\n"
    "v_exp_f32_e64 v177, v177 clamp\n"
    "v_mfma_f32_16x16x32_bf16 v[40:43], v[192:195], v[112:115], v[152:155]\n"
    "v_exp_f32_e64 v180, v180 clamp\n"
    "v_mfma_f32_16x16x32_bf16 v[44:47], v[192:195], v[120:123], v[156:159]\n"
    "v_exp_f32_e64 v181, v181 clamp\n"
    "s_waitcnt lgkmcnt(0)\n"
    "v_mfma_f32_16x16x32_bf16 v[32:35], v[196:199], v[100:103], v[32:35]\n"
    "v_exp_f32_e64 v184, v184 clamp\n"
    "v_mfma_f32_16x16x32_bf16 v[36:39], v[196:199], v[108:111], v[36:39]\n"
    "v_exp_f32_e64 v185, v185 clamp\n"
    "v_mfma_f32_16x16x32_bf16 v[40:43], v[196:199], v[116:119], v[40:43]\n"
    "v_exp_f32_e64 v188, v188 clamp\n"
    "v_mfma_f32_16x16x32_bf16 v[44:47], v[196:199], v[124:127], v[44:47]\n"
    "v_exp_f32_e64 v189, v189 clamp\n"
    "s_setprio 1\n"
    "s_waitcnt vmcnt(0)\n"
    "ds_read_b128 v[200:203], v8 offset:9728\n"
    "ds_read_b128 v[204:207], v9 offset:9728\n"
    "v_pk_add_f32 v[176:177], v[176:177], s[40:41] op_sel_hi:[1,0]\n"
    "v_pk_add_f32 v[180:181], v[180:181], s[40:41] op_sel_hi:[1,0]\n"
    "v_pk_add_f32 v[184:185], v[184:185], s[40:41] op_sel_hi:[1,0]\n"
    "v_pk_add_f32 v[188:189], v[188:189], s[40:41] op_sel_hi:[1,0]\n"
    "v_pk_mul_f32 v[4:5], v[176:177], v[180:181]\n"
    "v_pk_mul_f32 v[6:7], v[184:185], v[188:189]\n"
    "v_pk_mul_f32 v[176:177], v[176:177], v[28:29] op_sel_hi:[1,0]\n"
    "v_rcp_f32 v4, v4\n"
    "v_rcp_f32 v5, v5\n"
    "v_pk_mul_f32 v[184:185], v[184:185], v[0:1] op_sel_hi:[1,0]\n"
    "v_pk_fma_f32 v[176:177], v[180:181], v[252:253], v[176:177] op_sel_hi:[1,0,1]\n"
    "v_rcp_f32 v6, v6\n"
    "v_rcp_f32 v7, v7\n"
    "v_pk_fma_f32 v[176:177], v[176:177], v[4:5], v[2:3] op_sel_hi:[1,1,0]\n"
    "v_pk_fma_f32 v[184:185], v[188:189], v[254:255], v[184:185] op_sel_hi:[1,0,1]\n"
    "v_pk_fma_f32 v[176:177], v[184:185], v[6:7], v[176:177]\n"
    "v_pk_mul_f32 v[176:177], v[176:177], v[244:245]\n"
    "v_cvt_pk_bf16_f32 v250, v176, v177\n"
    "s_waitcnt lgkmcnt(1)\n"
    "v_mfma_f32_16x16x32_bf16 v[48:51], v[200:203], v[96:99], v[144:147]\n"
    "v_exp_f32_e64 v32, v32 clamp\n"
    "v_mfma_f32_16x16x32_bf16 v[52:55], v[200:203], v[104:107], v[148:151]\n"
    "v_exp_f32_e64 v33, v33 clamp\n"
    "v_mfma_f32_16x16x32_bf16 v[56:59], v[200:203], v[112:115], v[152:155]\n"
    "v_exp_f32_e64 v36, v36 clamp\n"
    "v_mfma_f32_16x16x32_bf16 v[60:63], v[200:203], v[120:123], v[156:159]\n"
    "v_exp_f32_e64 v37, v37 clamp\n"
    "s_waitcnt lgkmcnt(0)\n"
    "v_mfma_f32_16x16x32_bf16 v[48:51], v[204:207], v[100:103], v[48:51]\n"
    "v_exp_f32_e64 v40, v40 clamp\n"
    "v_mfma_f32_16x16x32_bf16 v[52:55], v[204:207], v[108:111], v[52:55]\n"
    "v_exp_f32_e64 v41, v41 clamp\n"
    "v_mfma_f32_16x16x32_bf16 v[56:59], v[204:207], v[116:119], v[56:59]\n"
    "v_exp_f32_e64 v44, v44 clamp\n"
    "v_mfma_f32_16x16x32_bf16 v[60:63], v[204:207], v[124:127], v[60:63]\n"
    "v_exp_f32_e64 v45, v45 clamp\n"
    "v_mfma_f32_16x16x32_bf16 v[224:227], v[248:251], v[208:211], 0\n"
    "v_exp_f32_e64 v34, v34 clamp\n"
    "v_mfma_f32_16x16x32_bf16 v[228:231], v[248:251], v[212:215], 0\n"
    "v_exp_f32_e64 v35, v35 clamp\n"
    "v_mfma_f32_16x16x32_bf16 v[232:235], v[248:251], v[216:219], 0\n"
    "v_exp_f32_e64 v38, v38 clamp\n"
    "v_mfma_f32_16x16x32_bf16 v[236:239], v[248:251], v[220:223], 0\n"
    "v_exp_f32_e64 v39, v39 clamp\n"
    "v_exp_f32_e64 v42, v42 clamp\n"
    "v_exp_f32_e64 v43, v43 clamp\n"
    "v_exp_f32_e64 v46, v46 clamp\n"
    "v_exp_f32_e64 v47, v47 clamp\n"
    "ds_read_b64_tr_b16 v[208:209], v10 offset:7680\n"
    "ds_read_b64_tr_b16 v[210:211], v10 offset:9728\n"
    "ds_read_b64_tr_b16 v[212:213], v11 offset:7680\n"
    "ds_read_b64_tr_b16 v[214:215], v11 offset:9728\n"
    "ds_read_b64_tr_b16 v[216:217], v12 offset:7680\n"
    "ds_read_b64_tr_b16 v[218:219], v12 offset:9728\n"
    "ds_read_b64_tr_b16 v[220:221], v13 offset:7680\n"
    "ds_read_b64_tr_b16 v[222:223], v13 offset:9728\n"
    "v_pk_add_f32 v[32:33], v[32:33], s[40:41] op_sel_hi:[1,0]\n"
    "v_pk_add_f32 v[36:37], v[36:37], s[40:41] op_sel_hi:[1,0]\n"
    "v_pk_add_f32 v[40:41], v[40:41], s[40:41] op_sel_hi:[1,0]\n"
    "v_pk_add_f32 v[44:45], v[44:45], s[40:41] op_sel_hi:[1,0]\n"
    "v_pk_mul_f32 v[4:5], v[32:33], v[36:37]\n"
    "v_pk_mul_f32 v[6:7], v[40:41], v[44:45]\n"
    "v_pk_mul_f32 v[32:33], v[32:33], v[28:29] op_sel_hi:[1,0]\n"
    "v_rcp_f32 v4, v4\n"
    "v_rcp_f32 v5, v5\n"
    "v_pk_mul_f32 v[40:41], v[40:41], v[0:1] op_sel_hi:[1,0]\n"
    "v_pk_fma_f32 v[32:33], v[36:37], v[252:253], v[32:33] op_sel_hi:[1,0,1]\n"
    "v_rcp_f32 v6, v6\n"
    "v_rcp_f32 v7, v7\n"
    "v_pk_fma_f32 v[32:33], v[32:33], v[4:5], v[2:3] op_sel_hi:[1,1,0]\n"
    "v_pk_fma_f32 v[40:41], v[44:45], v[254:255], v[40:41] op_sel_hi:[1,0,1]\n"
    "v_pk_fma_f32 v[32:33], v[40:41], v[6:7], v[32:33]\n"
    "v_pk_mul_f32 v[32:33], v[32:33], v[16:17]\n"
    "v_cvt_pk_bf16_f32 v24, v32, v33\n"
    "v_pk_add_f32 v[34:35], v[34:35], s[40:41] op_sel_hi:[1,0]\n"
    "v_pk_add_f32 v[38:39], v[38:39], s[40:41] op_sel_hi:[1,0]\n"
    "v_pk_add_f32 v[42:43], v[42:43], s[40:41] op_sel_hi:[1,0]\n"
    "v_pk_add_f32 v[46:47], v[46:47], s[40:41] op_sel_hi:[1,0]\n"
    "v_pk_mul_f32 v[4:5], v[34:35], v[38:39]\n"
    "v_pk_mul_f32 v[6:7], v[42:43], v[46:47]\n"
    "v_pk_mul_f32 v[34:35], v[34:35], v[28:29] op_sel_hi:[1,0]\n"
    "v_rcp_f32 v4, v4\n"
    "v_rcp_f32 v5, v5\n"
    "v_pk_mul_f32 v[42:43], v[42:43], v[0:1] op_sel_hi:[1,0]\n"
    "v_pk_fma_f32 v[34:35], v[38:39], v[252:253], v[34:35] op_sel_hi:[1,0,1]\n"
    "v_rcp_f32 v6, v6\n"
    "v_rcp_f32 v7, v7\n"
    "v_pk_fma_f32 v[34:35], v[34:35], v[4:5], v[2:3] op_sel_hi:[1,1,0]\n"
    "v_pk_fma_f32 v[42:43], v[46:47], v[254:255], v[42:43] op_sel_hi:[1,0,1]\n"
    "v_pk_fma_f32 v[34:35], v[42:43], v[6:7], v[34:35]\n"
    "v_pk_mul_f32 v[34:35], v[34:35], v[18:19]\n"
    "v_cvt_pk_bf16_f32 v25, v34, v35\n"
    "s_cmp_le_u32 s30, 24\n"
    "v_pk_add_f32 v[224:225], v[224:225], v[226:227]\n"
    "v_pk_add_f32 v[228:229], v[228:229], v[230:231]\n"
    "v_pk_add_f32 v[232:233], v[232:233], v[234:235]\n"
    "v_pk_add_f32 v[236:237], v[236:237], v[238:239]\n"
    "v_add_f32 v226, v224, v225\n"
    "v_add_f32 v227, v228, v229\n"
    "v_add_f32 v230, v232, v233\n"
    "v_add_f32 v231, v236, v237\n"
    "s_nop 0\n"
    "v_permlane32_swap_b32 v226, v230\n"
    "v_permlane32_swap_b32 v227, v231\n"
    "v_pk_add_f32 v[226:227], v[226:227], v[230:231]\n"
    "s_nop 1\n"
    "v_permlane16_swap_b32 v226, v227\n"
    "v_add_f32 v226, v226, v227\n"
    "v_add_f32 v29, v29, v226\n"
    "s_cbranch_scc1 L_skipB%=\n"
    "s_cmp_le_u32 s30, 32\n"
    "v_exp_f32_e64 v50, v50 clamp\n"
    "v_exp_f32_e64 v51, v51 clamp\n"
    "v_exp_f32_e64 v54, v54 clamp\n"
    "v_exp_f32_e64 v55, v55 clamp\n"
    "v_exp_f32_e64 v58, v58 clamp\n"
    "v_exp_f32_e64 v59, v59 clamp\n"
    "v_exp_f32_e64 v62, v62 clamp\n"
    "v_exp_f32_e64 v63, v63 clamp\n"
    "v_pk_add_f32 v[50:51], v[50:51], s[40:41] op_sel_hi:[1,0]\n"
    "v_pk_add_f32 v[54:55], v[54:55], s[40:41] op_sel_hi:[1,0]\n"
    "v_pk_add_f32 v[58:59], v[58:59], s[40:41] op_sel_hi:[1,0]\n"
    "v_pk_add_f32 v[62:63], v[62:63], s[40:41] op_sel_hi:[1,0]\n"
    "v_pk_mul_f32 v[4:5], v[50:51], v[54:55]\n"
    "v_pk_mul_f32 v[6:7], v[58:59], v[62:63]\n"
    "v_pk_mul_f32 v[50:51], v[50:51], v[28:29] op_sel_hi:[1,0]\n"
    "v_rcp_f32 v4, v4\n"
    "v_rcp_f32 v5, v5\n"
    "v_pk_mul_f32 v[58:59], v[58:59], v[0:1] op_sel_hi:[1,0]\n"
    "v_pk_fma_f32 v[50:51], v[54:55], v[252:253], v[50:51] op_sel_hi:[1,0,1]\n"
    "v_rcp_f32 v6, v6\n"
    "v_rcp_f32 v7, v7\n"
    "v_pk_fma_f32 v[50:51], v[50:51], v[4:5], v[2:3] op_sel_hi:[1,1,0]\n"
    "v_pk_fma_f32 v[58:59], v[62:63], v[254:255], v[58:59] op_sel_hi:[1,0,1]\n"
    "v_pk_fma_f32 v[50:51], v[58:59], v[6:7], v[50:51]\n"
    "v_pk_mul_f32 v[50:51], v[50:51], v[22:23]\n"
    "v_cvt_pk_bf16_f32 v27, v50, v51\n"
    "s_cbranch_scc1 L_skipB%=\n"
    "ds_read_b128 v[200:203], v8 offset:10752\n"
    "ds_read_b128 v[204:207], v9 offset:10752\n"
    "ds_read_b64 v[186:187], v14 offset:53632\n"
    "ds_read_b64_tr_b16 v[176:177], v10 offset:10752\n"
    "ds_read_b64_tr_b16 v[178:179], v11 offset:10752\n"
    "ds_read_b64_tr_b16 v[180:181], v12 offset:10752\n"
    "ds_read_b64_tr_b16 v[182:183], v13 offset:10752\n"
    "v_mov_b32 v188, 0\n"
    "s_waitcnt lgkmcnt(6)\n"
    "v_mfma_f32_16x16x32_bf16 v[160:163], v[200:203], v[96:99], v[144:147]\n"
    "v_mfma_f32_16x16x32_bf16 v[164:167], v[200:203], v[104:107], v[148:151]\n"
    "v_mfma_f32_16x16x32_bf16 v[168:171], v[200:203], v[112:115], v[152:155]\n"
    "v_mfma_f32_16x16x32_bf16 v[172:175], v[200:203], v[120:123], v[156:159]\n"
    "s_waitcnt lgkmcnt(5)\n"
    "v_mfma_f32_16x16x32_bf16 v[160:163], v[204:207], v[100:103], v[160:163]\n"
    "v_mfma_f32_16x16x32_bf16 v[164:167], v[204:207], v[108:111], v[164:167]\n"
    "v_mfma_f32_16x16x32_bf16 v[168:171], v[204:207], v[116:119], v[168:171]\n"
    "v_mfma_f32_16x16x32_bf16 v[172:175], v[204:207], v[124:127], v[172:175]\n"
    "s_nop 4\n"
    "v_exp_f32_e64 v162, v162 clamp\n"
    "v_exp_f32_e64 v163, v163 clamp\n"
    "v_exp_f32_e64 v166, v166 clamp\n"
    "v_exp_f32_e64 v167, v167 clamp\n"
    "v_exp_f32_e64 v170, v170 clamp\n"
    "v_exp_f32_e64 v171, v171 clamp\n"
    "v_exp_f32_e64 v174, v174 clamp\n"
    "v_exp_f32_e64 v175, v175 clamp\n"
    "v_pk_add_f32 v[162:163], v[162:163], s[40:41] op_sel_hi:[1,0]\n"
    "v_pk_add_f32 v[166:167], v[166:167], s[40:41] op_sel_hi:[1,0]\n"
    "v_pk_add_f32 v[170:171], v[170:171], s[40:41] op_sel_hi:[1,0]\n"
    "v_pk_add_f32 v[174:175], v[174:175], s[40:41] op_sel_hi:[1,0]\n"
    "v_pk_mul_f32 v[4:5], v[162:163], v[166:167]\n"
    "v_pk_mul_f32 v[6:7], v[170:171], v[174:175]\n"
    "v_pk_mul_f32 v[162:163], v[162:163], v[28:29] op_sel_hi:[1,0]\n"
    "v_rcp_f32 v4, v4\n"
    "v_rcp_f32 v5, v5\n"
    "v_pk_mul_f32 v[170:171], v[170:171], v[0:1] op_sel_hi:[1,0]\n"
    "v_pk_fma_f32 v[162:163], v[166:167], v[252:253], v[162:163] op_sel_hi:[1,0,1]\n"
    "v_rcp_f32 v6, v6\n"
    "v_rcp_f32 v7, v7\n"
    "v_pk_fma_f32 v[162:163], v[162:163], v[4:5], v[2:3] op_sel_hi:[1,1,0]\n"
    "v_pk_fma_f32 v[170:171], v[174:175], v[254:255], v[170:171] op_sel_hi:[1,0,1]\n"
    "v_pk_fma_f32 v[162:163], v[170:171], v[6:7], v[162:163]\n"
    "s_waitcnt lgkmcnt(4)\n"
    "v_pk_mul_f32 v[162:163], v[162:163], v[186:187]\n"
    "v_cvt_pk_bf16_f32 v189, v162, v163\n"
    "s_waitcnt lgkmcnt(3)\n"
    "s_nop 0\n"
    "v_mfma_f32_16x16x16_bf16 v[224:227], v[188:189], v[176:177], 0\n"
    "s_waitcnt lgkmcnt(2)\n"
    "v_mfma_f32_16x16x16_bf16 v[228:231], v[188:189], v[178:179], 0\n"
    "s_waitcnt lgkmcnt(1)\n"
    "v_mfma_f32_16x16x16_bf16 v[232:235], v[188:189], v[180:181], 0\n"
    "s_waitcnt lgkmcnt(0)\n"
    "v_mfma_f32_16x16x16_bf16 v[236:239], v[188:189], v[182:183], 0\n"
    "s_nop 1\n"
    "v_pk_add_f32 v[224:225], v[224:225], v[226:227]\n"
    "s_nop 0\n"
    "v_pk_add_f32 v[228:229], v[228:229], v[230:231]\n"
    "s_nop 0\n"
    "v_pk_add_f32 v[232:233], v[232:233], v[234:235]\n"
    "s_nop 0\n"
    "v_pk_add_f32 v[236:237], v[236:237], v[238:239]\n"
    "v_add_f32 v226, v224, v225\n"
    "v_add_f32 v227, v228, v229\n"
    "v_add_f32 v230, v232, v233\n"
    "v_add_f32 v231, v236, v237\n"
    "s_nop 0\n"
    "v_permlane32_swap_b32 v226, v230\n"
    "v_permlane32_swap_b32 v227, v231\n"
    "v_pk_add_f32 v[226:227], v[226:227], v[230:231]\n"
    "s_nop 1\n"
    "v_permlane16_swap_b32 v226, v227\n"
    "v_add_f32 v226, v226, v227\n"
    "v_add_f32 v30, v30, v226\n"
    "L_skipB%=:\n"
    "s_setprio 0\n"
    "s_cmp_gt_u32 s48, 40\n"
    "v_exp_f32_e64 v48, v48 clamp\n"
    "v_exp_f32_e64 v49, v49 clamp\n"
    "v_exp_f32_e64 v52, v52 clamp\n"
    "v_exp_f32_e64 v53, v53 clamp\n"
    "v_exp_f32_e64 v56, v56 clamp\n"
    "v_exp_f32_e64 v57, v57 clamp\n"
    "v_exp_f32_e64 v60, v60 clamp\n"
    "v_exp_f32_e64 v61, v61 clamp\n"
    "v_pk_add_f32 v[48:49], v[48:49], s[40:41] op_sel_hi:[1,0]\n"
    "v_pk_add_f32 v[52:53], v[52:53], s[40:41] op_sel_hi:[1,0]\n"
    "v_pk_add_f32 v[56:57], v[56:57], s[40:41] op_sel_hi:[1,0]\n"
    "v_pk_add_f32 v[60:61], v[60:61], s[40:41] op_sel_hi:[1,0]\n"
    "v_pk_mul_f32 v[4:5], v[48:49], v[52:53]\n"
    "v_pk_mul_f32 v[6:7], v[56:57], v[60:61]\n"
    "v_pk_mul_f32 v[48:49], v[48:49], v[28:29] op_sel_hi:[1,0]\n"
    "v_rcp_f32 v4, v4\n"
    "v_rcp_f32 v5, v5\n"
    "v_pk_mul_f32 v[56:57], v[56:57], v[0:1] op_sel_hi:[1,0]\n"
    "v_pk_fma_f32 v[48:49], v[52:53], v[252:253], v[48:49] op_sel_hi:[1,0,1]\n"
    "v_rcp_f32 v6, v6\n"
    "v_rcp_f32 v7, v7\n"
    "v_pk_fma_f32 v[48:49], v[48:49], v[4:5], v[2:3] op_sel_hi:[1,1,0]\n"
    "v_pk_fma_f32 v[56:57], v[60:61], v[254:255], v[56:57] op_sel_hi:[1,0,1]\n"
    "v_pk_fma_f32 v[48:49], v[56:57], v[6:7], v[48:49]\n"
    "v_pk_mul_f32 v[48:49], v[48:49], v[20:21]\n"
    "v_cvt_pk_bf16_f32 v26, v48, v49\n"
    "s_waitcnt lgkmcnt(0)\n"
    "s_nop 0\n"
    "v_mfma_f32_16x16x32_bf16 v[224:227], v[24:27], v[208:211], 0\n"
    "v_mfma_f32_16x16x32_bf16 v[228:231], v[24:27], v[212:215], 0\n"
    "v_mfma_f32_16x16x32_bf16 v[232:235], v[24:27], v[216:219], 0\n"
    "v_mfma_f32_16x16x32_bf16 v[236:239], v[24:27], v[220:223], 0\n"
    "s_nop 4\n"
    "v_pk_add_f32 v[224:225], v[224:225], v[226:227]\n"
    "v_pk_add_f32 v[228:229], v[228:229], v[230:231]\n"
    "v_pk_add_f32 v[232:233], v[232:233], v[234:235]\n"
    "v_pk_add_f32 v[236:237], v[236:237], v[238:239]\n"
    "v_add_f32 v226, v224, v225\n"
    "v_add_f32 v227, v228, v229\n"
    "v_add_f32 v230, v232, v233\n"
    "v_add_f32 v231, v236, v237\n"
    "s_nop 0\n"
    "v_permlane32_swap_b32 v226, v230\n"
    "v_permlane32_swap_b32 v227, v231\n"
    "v_pk_add_f32 v[226:227], v[226:227], v[230:231]\n"
    "s_nop 1\n"
    "v_permlane16_swap_b32 v226, v227\n"
    "v_add_f32 v226, v226, v227\n"
    "v_add_f32 v30, v30, v226\n"
    "s_cbranch_scc1 L_tail%=\n"
    "L_tail_ret%=:\n"
    "v_mul_f32 v29, s40, v29\n"
    "v_mul_f32 v30, s40, v30\n"
    "v_cvt_pk_bf16_f32 v224, v29, v30\n"
    "global_store_short v15, v224, s[22:23] sc0 sc1\n"
    "global_store_short_d16_hi v15, v224, s[22:23] offset:128 sc0 sc1\n"
    "s_endpgm\n"
    "L_tail%=:\n"
    "ds_read2_b32 v[32:33], v31 offset0:40 offset1:48\n"
    "ds_read2_b32 v[34:35], v31 offset0:104 offset1:112\n"
    "v_lshlrev_b32 v36, 4, v1\n"
    "v_lshlrev_b32 v37, 1, v1\n"
    "v_and_b32 v37, 32, v37\n"
    "v_xor_b32 v36, v36, v37\n"
    "v_and_b32 v36, 0x70, v36\n"
    "v_xor_b32 v37, 64, v36\n"
    "s_add_u32 m0, s46, 0\n"
    "s_waitcnt lgkmcnt(1)\n"
    "v_or_b32 v32, v32, v36\n"
    "buffer_load_dwordx4 v32, s[24:27], 0 offen nt lds\n"
    "s_add_u32 m0, s46, 1024\n"
    "v_or_b32 v33, v33, v37\n"
    "s_mov_b64 exec, 0xffff\n"
    "buffer_load_dwordx4 v33, s[24:27], 0 offen nt lds\n"
    "s_mov_b64 exec, -1\n"
    "s_add_u32 m0, s46, 6400\n"
    "s_waitcnt lgkmcnt(0)\n"
    "v_or_b32 v34, v34, v36\n"
    "buffer_load_dwordx4 v34, s[24:27], 0 offen nt lds\n"
    "s_add_u32 m0, s46, 7424\n"
    "v_or_b32 v35, v35, v37\n"
    "s_mov_b64 exec, 0xffff\n"
    "buffer_load_dwordx4 v35, s[24:27], 0 offen nt lds\n"
    "s_mov_b64 exec, -1\n"
    "s_waitcnt vmcnt(0)\n"
    "ds_read_b128 v[200:203], v8 offset:0\n"
    "ds_read_b128 v[204:207], v9 offset:0\n"
    "ds_read_b64 v[184:185], v14 offset:53408\n"
    "ds_read_b64 v[186:187], v14 offset:53440\n"
    "ds_read_b64_tr_b16 v[176:177], v10 offset:0\n"
    "ds_read_b64_tr_b16 v[178:179], v11 offset:0\n"
    "ds_read_b64_tr_b16 v[180:181], v12 offset:0\n"
    "ds_read_b64_tr_b16 v[182:183], v13 offset:0\n"
    "v_mov_b32 v188, 0\n"
    "s_waitcnt lgkmcnt(7)\n"
    "v_mfma_f32_16x16x32_bf16 v[160:163], v[200:203], v[64:67], v[128:131]\n"
    "v_mfma_f32_16x16x32_bf16 v[164:167], v[200:203], v[72:75], v[132:135]\n"
    "v_mfma_f32_16x16x32_bf16 v[168:171], v[200:203], v[80:83], v[136:139]\n"
    "v_mfma_f32_16x16x32_bf16 v[172:175], v[200:203], v[88:91], v[140:143]\n"
    "s_waitcnt lgkmcnt(6)\n"
    "v_mfma_f32_16x16x32_bf16 v[160:163], v[204:207], v[68:71], v[160:163]\n"
    "v_mfma_f32_16x16x32_bf16 v[164:167], v[204:207], v[76:79], v[164:167]\n"
    "v_mfma_f32_16x16x32_bf16 v[168:171], v[204:207], v[84:87], v[168:171]\n"
    "v_mfma_f32_16x16x32_bf16 v[172:175], v[204:207], v[92:95], v[172:175]\n"
    "s_nop 4\n"
    "v_exp_f32_e64 v160, v160 clamp\n"
    "v_exp_f32_e64 v161, v161 clamp\n"
    "v_exp_f32_e64 v164, v164 clamp\n"
    "v_exp_f32_e64 v165, v165 clamp\n"
    "v_exp_f32_e64 v168, v168 clamp\n"
    "v_exp_f32_e64 v169, v169 clamp\n"
    "v_exp_f32_e64 v172, v172 clamp\n"
    "v_exp_f32_e64 v173, v173 clamp\n"
    "v_pk_add_f32 v[160:161], v[160:161], s[40:41] op_sel_hi:[1,0]\n"
    "v_pk_add_f32 v[164:165], v[164:165], s[40:41] op_sel_hi:[1,0]\n"
    "v_pk_add_f32 v[168:169], v[168:169], s[40:41] op_sel_hi:[1,0]\n"
    "v_pk_add_f32 v[172:173], v[172:173], s[40:41] op_sel_hi:[1,0]\n"
    "v_pk_mul_f32 v[4:5], v[160:161], v[164:165]\n"
    "v_pk_mul_f32 v[6:7], v[168:169], v[172:173]\n"
    "v_pk_mul_f32 v[160:161], v[160:161], v[28:29] op_sel_hi:[1,0]\n"
    "v_rcp_f32 v4, v4\n"
    "v_rcp_f32 v5, v5\n"
    "v_pk_mul_f32 v[168:169], v[168:169], v[0:1] op_sel_hi:[1,0]\n"
    "v_pk_fma_f32 v[160:161], v[164:165], v[252:253], v[160:161] op_sel_hi:[1,0,1]\n"
    "v_rcp_f32 v6, v6\n"
    "v_rcp_f32 v7, v7\n"
    "v_pk_fma_f32 v[160:161], v[160:161], v[4:5], v[2:3] op_sel_hi:[1,1,0]\n"
    "v_pk_fma_f32 v[168:169], v[172:173], v[254:255], v[168:169] op_sel_hi:[1,0,1]\n"
    "v_pk_fma_f32 v[160:161], v[168:169], v[6:7], v[160:161]\n"
    "s_waitcnt lgkmcnt(5)\n"
    "v_pk_mul_f32 v[160:161], v[160:161], v[184:185]\n"
    "v_cvt_pk_bf16_f32 v188, v160, v161\n"
    "v_exp_f32_e64 v162, v162 clamp\n"
    "v_exp_f32_e64 v163, v163 clamp\n"
    "v_exp_f32_e64 v166, v166 clamp\n"
    "v_exp_f32_e64 v167, v167 clamp\n"
    "v_exp_f32_e64 v170, v170 clamp\n"
    "v_exp_f32_e64 v171, v171 clamp\n"
    "v_exp_f32_e64 v174, v174 clamp\n"
    "v_exp_f32_e64 v175, v175 clamp\n"
    "v_pk_add_f32 v[162:163], v[162:163], s[40:41] op_sel_hi:[1,0]\n"
    "v_pk_add_f32 v[166:167], v[166:167], s[40:41] op_sel_hi:[1,0]\n"
    "v_pk_add_f32 v[170:171], v[170:171], s[40:41] op_sel_hi:[1,0]\n"
    "v_pk_add_f32 v[174:175], v[174:175], s[40:41] op_sel_hi:[1,0]\n"
    "v_pk_mul_f32 v[4:5], v[162:163], v[166:167]\n"
    "v_pk_mul_f32 v[6:7], v[170:171], v[174:175]\n"
    "v_pk_mul_f32 v[162:163], v[162:163], v[28:29] op_sel_hi:[1,0]\n"
    "v_rcp_f32 v4, v4\n"
    "v_rcp_f32 v5, v5\n"
    "v_pk_mul_f32 v[170:171], v[170:171], v[0:1] op_sel_hi:[1,0]\n"
    "v_pk_fma_f32 v[162:163], v[166:167], v[252:253], v[162:163] op_sel_hi:[1,0,1]\n"
    "v_rcp_f32 v6, v6\n"
    "v_rcp_f32 v7, v7\n"
    "v_pk_fma_f32 v[162:163], v[162:163], v[4:5], v[2:3] op_sel_hi:[1,1,0]\n"
    "v_pk_fma_f32 v[170:171], v[174:175], v[254:255], v[170:171] op_sel_hi:[1,0,1]\n"
    "v_pk_fma_f32 v[162:163], v[170:171], v[6:7], v[162:163]\n"
    "s_waitcnt lgkmcnt(4)\n"
    "v_pk_mul_f32 v[162:163], v[162:163], v[186:187]\n"
    "v_cvt_pk_bf16_f32 v189, v162, v163\n"
    "s_waitcnt lgkmcnt(3)\n"
    "s_nop 0\n"
    "v_mfma_f32_16x16x16_bf16 v[224:227], v[188:189], v[176:177], 0\n"
    "s_waitcnt lgkmcnt(2)\n"
    "v_mfma_f32_16x16x16_bf16 v[228:231], v[188:189], v[178:179], 0\n"
    "s_waitcnt lgkmcnt(1)\n"
    "v_mfma_f32_16x16x16_bf16 v[232:235], v[188:189], v[180:181], 0\n"
    "s_waitcnt lgkmcnt(0)\n"
    "v_mfma_f32_16x16x16_bf16 v[236:239], v[188:189], v[182:183], 0\n"
    "s_nop 1\n"
    "v_pk_add_f32 v[224:225], v[224:225], v[226:227]\n"
    "s_nop 0\n"
    "v_pk_add_f32 v[228:229], v[228:229], v[230:231]\n"
    "s_nop 0\n"
    "v_pk_add_f32 v[232:233], v[232:233], v[234:235]\n"
    "s_nop 0\n"
    "v_pk_add_f32 v[236:237], v[236:237], v[238:239]\n"
    "v_add_f32 v226, v224, v225\n"
    "v_add_f32 v227, v228, v229\n"
    "v_add_f32 v230, v232, v233\n"
    "v_add_f32 v231, v236, v237\n"
    "s_nop 0\n"
    "v_permlane32_swap_b32 v226, v230\n"
    "v_permlane32_swap_b32 v227, v231\n"
    "v_pk_add_f32 v[226:227], v[226:227], v[230:231]\n"
    "s_nop 1\n"
    "v_permlane16_swap_b32 v226, v227\n"
    "v_add_f32 v226, v226, v227\n"
    "v_add_f32 v29, v29, v226\n"
    "ds_read_b128 v[200:203], v8 offset:6400\n"
    "ds_read_b128 v[204:207], v9 offset:6400\n"
    "ds_read_b64 v[184:185], v14 offset:53664\n"
    "ds_read_b64 v[186:187], v14 offset:53696\n"
    "ds_read_b64_tr_b16 v[176:177], v10 offset:6400\n"
    "ds_read_b64_tr_b16 v[178:179], v11 offset:6400\n"
    "ds_read_b64_tr_b16 v[180:181], v12 offset:6400\n"
    "ds_read_b64_tr_b16 v[182:183], v13 offset:6400\n"
    "v_mov_b32 v188, 0\n"
    "s_waitcnt lgkmcnt(7)\n"
    "v_mfma_f32_16x16x32_bf16 v[160:163], v[200:203], v[96:99], v[144:147]\n"
    "v_mfma_f32_16x16x32_bf16 v[164:167], v[200:203], v[104:107], v[148:151]\n"
    "v_mfma_f32_16x16x32_bf16 v[168:171], v[200:203], v[112:115], v[152:155]\n"
    "v_mfma_f32_16x16x32_bf16 v[172:175], v[200:203], v[120:123], v[156:159]\n"
    "s_waitcnt lgkmcnt(6)\n"
    "v_mfma_f32_16x16x32_bf16 v[160:163], v[204:207], v[100:103], v[160:163]\n"
    "v_mfma_f32_16x16x32_bf16 v[164:167], v[204:207], v[108:111], v[164:167]\n"
    "v_mfma_f32_16x16x32_bf16 v[168:171], v[204:207], v[116:119], v[168:171]\n"
    "v_mfma_f32_16x16x32_bf16 v[172:175], v[204:207], v[124:127], v[172:175]\n"
    "s_nop 4\n"
    "v_exp_f32_e64 v160, v160 clamp\n"
    "v_exp_f32_e64 v161, v161 clamp\n"
    "v_exp_f32_e64 v164, v164 clamp\n"
    "v_exp_f32_e64 v165, v165 clamp\n"
    "v_exp_f32_e64 v168, v168 clamp\n"
    "v_exp_f32_e64 v169, v169 clamp\n"
    "v_exp_f32_e64 v172, v172 clamp\n"
    "v_exp_f32_e64 v173, v173 clamp\n"
    "v_pk_add_f32 v[160:161], v[160:161], s[40:41] op_sel_hi:[1,0]\n"
    "v_pk_add_f32 v[164:165], v[164:165], s[40:41] op_sel_hi:[1,0]\n"
    "v_pk_add_f32 v[168:169], v[168:169], s[40:41] op_sel_hi:[1,0]\n"
    "v_pk_add_f32 v[172:173], v[172:173], s[40:41] op_sel_hi:[1,0]\n"
    "v_pk_mul_f32 v[4:5], v[160:161], v[164:165]\n"
    "v_pk_mul_f32 v[6:7], v[168:169], v[172:173]\n"
    "v_pk_mul_f32 v[160:161], v[160:161], v[28:29] op_sel_hi:[1,0]\n"
    "v_rcp_f32 v4, v4\n"
    "v_rcp_f32 v5, v5\n"
    "v_pk_mul_f32 v[168:169], v[168:169], v[0:1] op_sel_hi:[1,0]\n"
    "v_pk_fma_f32 v[160:161], v[164:165], v[252:253], v[160:161] op_sel_hi:[1,0,1]\n"
    "v_rcp_f32 v6, v6\n"
    "v_rcp_f32 v7, v7\n"
    "v_pk_fma_f32 v[160:161], v[160:161], v[4:5], v[2:3] op_sel_hi:[1,1,0]\n"
    "v_pk_fma_f32 v[168:169], v[172:173], v[254:255], v[168:169] op_sel_hi:[1,0,1]\n"
    "v_pk_fma_f32 v[160:161], v[168:169], v[6:7], v[160:161]\n"
    "s_waitcnt lgkmcnt(5)\n"
    "v_pk_mul_f32 v[160:161], v[160:161], v[184:185]\n"
    "v_cvt_pk_bf16_f32 v188, v160, v161\n"
    "v_exp_f32_e64 v162, v162 clamp\n"
    "v_exp_f32_e64 v163, v163 clamp\n"
    "v_exp_f32_e64 v166, v166 clamp\n"
    "v_exp_f32_e64 v167, v167 clamp\n"
    "v_exp_f32_e64 v170, v170 clamp\n"
    "v_exp_f32_e64 v171, v171 clamp\n"
    "v_exp_f32_e64 v174, v174 clamp\n"
    "v_exp_f32_e64 v175, v175 clamp\n"
    "v_pk_add_f32 v[162:163], v[162:163], s[40:41] op_sel_hi:[1,0]\n"
    "v_pk_add_f32 v[166:167], v[166:167], s[40:41] op_sel_hi:[1,0]\n"
    "v_pk_add_f32 v[170:171], v[170:171], s[40:41] op_sel_hi:[1,0]\n"
    "v_pk_add_f32 v[174:175], v[174:175], s[40:41] op_sel_hi:[1,0]\n"
    "v_pk_mul_f32 v[4:5], v[162:163], v[166:167]\n"
    "v_pk_mul_f32 v[6:7], v[170:171], v[174:175]\n"
    "v_pk_mul_f32 v[162:163], v[162:163], v[28:29] op_sel_hi:[1,0]\n"
    "v_rcp_f32 v4, v4\n"
    "v_rcp_f32 v5, v5\n"
    "v_pk_mul_f32 v[170:171], v[170:171], v[0:1] op_sel_hi:[1,0]\n"
    "v_pk_fma_f32 v[162:163], v[166:167], v[252:253], v[162:163] op_sel_hi:[1,0,1]\n"
    "v_rcp_f32 v6, v6\n"
    "v_rcp_f32 v7, v7\n"
    "v_pk_fma_f32 v[162:163], v[162:163], v[4:5], v[2:3] op_sel_hi:[1,1,0]\n"
    "v_pk_fma_f32 v[170:171], v[174:175], v[254:255], v[170:171] op_sel_hi:[1,0,1]\n"
    "v_pk_fma_f32 v[162:163], v[170:171], v[6:7], v[162:163]\n"
    "s_waitcnt lgkmcnt(4)\n"
    "v_pk_mul_f32 v[162:163], v[162:163], v[186:187]\n"
    "v_cvt_pk_bf16_f32 v189, v162, v163\n"
    "s_waitcnt lgkmcnt(3)\n"
    "s_nop 0\n"
    "v_mfma_f32_16x16x16_bf16 v[224:227], v[188:189], v[176:177], 0\n"
    "s_waitcnt lgkmcnt(2)\n"
    "v_mfma_f32_16x16x16_bf16 v[228:231], v[188:189], v[178:179], 0\n"
    "s_waitcnt lgkmcnt(1)\n"
    "v_mfma_f32_16x16x16_bf16 v[232:235], v[188:189], v[180:181], 0\n"
    "s_waitcnt lgkmcnt(0)\n"
    "v_mfma_f32_16x16x16_bf16 v[236:239], v[188:189], v[182:183], 0\n"
    "s_nop 1\n"
    "v_pk_add_f32 v[224:225], v[224:225], v[226:227]\n"
    "s_nop 0\n"
    "v_pk_add_f32 v[228:229], v[228:229], v[230:231]\n"
    "s_nop 0\n"
    "v_pk_add_f32 v[232:233], v[232:233], v[234:235]\n"
    "s_nop 0\n"
    "v_pk_add_f32 v[236:237], v[236:237], v[238:239]\n"
    "v_add_f32 v226, v224, v225\n"
    "v_add_f32 v227, v228, v229\n"
    "v_add_f32 v230, v232, v233\n"
    "v_add_f32 v231, v236, v237\n"
    "s_nop 0\n"
    "v_permlane32_swap_b32 v226, v230\n"
    "v_permlane32_swap_b32 v227, v231\n"
    "v_pk_add_f32 v[226:227], v[226:227], v[230:231]\n"
    "s_nop 1\n"
    "v_permlane16_swap_b32 v226, v227\n"
    "v_add_f32 v226, v226, v227\n"
    "v_add_f32 v30, v30, v226\n"
    "s_branch L_tail_ret%=\n"
    "L_end%=:\n"
    :
    : "s"(__builtin_amdgcn_kernarg_segment_ptr()), "s"(blockIdx.x), "v"(tid), "s"((unsigned)(size_t)lds_raw)
    : "v1", "v2", "v3", "v4", "v5", "v6", "v7", "v8", "v9", "v10", "v11", "v12", "v13", "v14", "v15", "v16", "v17", "v18", "v19", "v20", "v21", "v22", "v23", "v24", "v25", "v26", "v27", "v28", "v29", "v30", "v31", "v32", "v33", "v34", "v35", "v36", "v37", "v38", "v39", "v40", "v41", "v42", "v43", "v44", "v45", "v46", "v47", "v48", "v49", "v50", "v51", "v52", "v53", "v54", "v55", "v56", "v57", "v58", "v59", "v60", "v61", "v62", "v63", "v64", "v65", "v66", "v67", "v68", "v69", "v70", "v71", "v72", "v73", "v74", "v75", "v76", "v77", "v78", "v79", "v80", "v81", "v82", "v83", "v84", "v85", "v86", "v87", "v88", "v89", "v90", "v91", "v92", "v93", "v94", "v95", "v96", "v97", "v98", "v99", "v100", "v101", "v102", "v103", "v104", "v105", "v106", "v107", "v108", "v109", "v110", "v111", "v112", "v113", "v114", "v115", "v116", "v117", "v118", "v119", "v120", "v121", "v122", "v123", "v124", "v125", "v126", "v127", "v128", "v129", "v130", "v131", "v132", "v133", "v134", "v135", "v136", "v137", "v138", "v139", "v140", "v141", "v142", "v143", "v144", "v145", "v146", "v147", "v148", "v149", "v150", "v151", "v152", "v153", "v154", "v155", "v156", "v157", "v158", "v159", "v160", "v161", "v162", "v163", "v164", "v165", "v166", "v167", "v168", "v169", "v170", "v171", "v172", "v173", "v174", "v175", "v176", "v177", "v178", "v179", "v180", "v181", "v182", "v183", "v184", "v185", "v186", "v187", "v188", "v189", "v190", "v191", "v192", "v193", "v194", "v195", "v196", "v197", "v198", "v199", "v200", "v201", "v202", "v203", "v204", "v205", "v206", "v207", "v208", "v209", "v210", "v211", "v212", "v213", "v214", "v215", "v216", "v217", "v218", "v219", "v220", "v221", "v222", "v223", "v224", "v225", "v226", "v227", "v228", "v229", "v230", "v231", "v232", "v233", "v234", "v235", "v236", "v237", "v238", "v239", "v240", "v241", "v242", "v243", "v244", "v245", "v246", "v247", "v248", "v249", "v250", "v251", "v252", "v253", "v254", "v255", "s8", "s9", "s10", "s11", "s12", "s13", "s14", "s15", "s16", "s17", "s18", "s19", "s20", "s21", "s22", "s23", "s24", "s25", "s26", "s27", "s28", "s29", "s30", "s31", "s34", "s35", "s36", "s37", "s38", "s39", "s40", "s41", "s42", "s43", "s44", "s45", "s46", "s47", "s48", "s49", "s50", "s51", "s52", "s53", "s54", "s55", "s56", "s57", "s58", "s59", "s60", "s61", "s62", "s63", "s64", "s65", "s66", "s67", "s68", "s69", "s70", "s71", "s72", "s73", "s74", "s75", "s76", "s77", "s78", "s79", "s80", "s81", "s82", "s83", "s84", "s85", "s86", "s87", "s88", "s89", "s90", "s91", "s92", "s93", "s94", "s95", "s96", "s97", "s98", "s99", "vcc", "scc", "memory");
}

extern "C" void kernel_launch(void* const* d_in, const int* in_sizes, int n_in, void* d_out, int out_size, void* d_ws, size_t ws_size,
                              hipStream_t stream) {
  const int B = in_sizes[0] / 64;
  din_kernel<<<B / 16, 512, 0, stream>>>((const unsigned short*)d_in[0], (const unsigned short*)d_in[1], (const unsigned short*)d_in[2],
                                         (const unsigned short*)d_in[3], (const unsigned short*)d_in[4], (const unsigned short*)d_in[5],
                                         (const unsigned short*)d_in[6], (unsigned short*)d_out);
}
```
